```python
import torch

def _softmax(x, axis=-1):
    return torch.softmax(x, dim=axis)
def _gelu(x):
    return torch.nn.functional.gelu(x, approximate="tanh")
def _silu(x):
    return torch.nn.functional.silu(x)
def _sigmoid(x):
    return torch.sigmoid(x)
def _layernorm(x, axis=-1, eps=1e-5):
    m = x.mean(dim=axis, keepdim=True)
    v = x.var(dim=axis, keepdim=True, unbiased=False)
    return (x - m) / torch.sqrt(v + eps)
def _scatter_add(tgt, idx, src, axis=0):
    return tgt.clone().index_add_(axis, idx, src)
def _segment_sum(src, seg, n):
    out = torch.zeros((n,) + src.shape[1:], dtype=src.dtype)
    return out.index_add_(0, seg, src)
def _pad(x, pw):
    flat = tuple(v for pair in reversed(pw) for v in pair)
    return torch.nn.functional.pad(x, flat)

kernel_name = 'standalone__paged_block_table_kv_radixattention__decode'

def setup_inputs():

    _w = (lambda a: (lambda t: t.to(torch.bfloat16) if t.is_floating_point() else t)(torch.as_tensor(a)))
    B, H, Hkv, Dh = 4, 32, 32, 128
    Lk = 8192
    bs = 16
    nb = max(2, Lk // bs)
    P = max(1, nb // 2)
    n_phys = P + B * (nb - P)
    shared = torch.arange(P, dtype=torch.int64)
    priv = P + torch.arange(B * (nb - P), dtype=torch.int64).reshape(B, nb - P)
    bt = torch.cat([torch.tile(shared[None, :], (B, 1)), priv], dim=1)
    ctx = nb * bs - (torch.arange(1, B + 1, dtype=torch.int64) * 2654435761) % max(1, (nb * bs - P * bs) // 2)
    return {
        "q": _w((lambda *s: torch.randn(*s, dtype=torch.bfloat16) * 0.8)(B, H, Dh)),
        "k_cache": _w((lambda *s: torch.randn(*s, dtype=torch.bfloat16) * 0.8)(n_phys, bs, Hkv, Dh)),
        "v_cache": _w((lambda *s: torch.randn(*s, dtype=torch.bfloat16) * 0.05)(n_phys, bs, Hkv, Dh)),
        "block_table": _w(bt.to(torch.int64)),
        "ctx_lens": _w(torch.as_tensor(ctx).to(torch.int64)),
    }

def reference(q, k_cache, v_cache, block_table, ctx_lens):
    B, H, Dh = q.shape
    n_phys, bs, Hkv, _ = k_cache.shape
    nb = block_table.shape[1]
    Lk = nb * bs
    rep = H // Hkv
    k = k_cache[block_table].reshape(B, Lk, Hkv, Dh)
    v = v_cache[block_table].reshape(B, Lk, Hkv, Dh)
    k = (lambda x, n, axis: torch.repeat_interleave(x, n, dim=axis))(k, rep, axis=2)
    v = (lambda x, n, axis: torch.repeat_interleave(x, n, dim=axis))(v, rep, axis=2)
    scores = torch.einsum("bhd,bkhd->bhk", q, k) * (Dh ** -0.5)
    pos = torch.arange(Lk)
    valid = pos[None, :] < ctx_lens[:, None]
    scores = torch.where(valid[:, None, :], scores, -1e9)
    p = _softmax(scores, axis=-1)
    return torch.einsum("bhk,bkhd->bhd", p, v)
```

```cpp
#include <hip/hip_runtime.h>
#include <hip/hip_bf16.h>
#include <stdint.h>

namespace {
constexpr int B = 4, H = 32, D = 128, BS = 16, NB = 512;
constexpr int NHG = 8;
constexpr int NSPLIT = 32;
constexpr int NWG = NHG * NSPLIT;
constexpr int NW = 4, NTHR = NW * 64;
constexpr int TOKB = H * D * 2;
constexpr int BLKB = BS * TOKB;
constexpr float C2 = 0.088388347648318440f * 1.4426950408889634f;
constexpr float MINIT = -1e30f;

typedef unsigned u32x4 __attribute__((ext_vector_type(4)));
typedef unsigned u32x2 __attribute__((ext_vector_type(2)));
typedef int i32x2 __attribute__((ext_vector_type(2)));
typedef float f32x4 __attribute__((ext_vector_type(4)));
typedef float f32x2 __attribute__((ext_vector_type(2)));
typedef short s16x4 __attribute__((ext_vector_type(4)));
typedef __bf16 bf16x2 __attribute__((ext_vector_type(2)));

__device__ __forceinline__ float dot2(unsigned k, unsigned q, float acc) {
  return __builtin_amdgcn_fdot2_f32_bf16(__builtin_bit_cast(bf16x2, k), __builtin_bit_cast(bf16x2, q), acc, false);
}
template <int CTRL> __device__ __forceinline__ float dpp(float x) {
  return __builtin_bit_cast(float, __builtin_amdgcn_mov_dpp(__builtin_bit_cast(int, x), CTRL, 0xf, 0xf, true));
}
__device__ __forceinline__ float rowsum16(float x) {
  x += dpp<0xB1>(x);
  x += dpp<0x4E>(x);
  x += dpp<0x141>(x);
  x += dpp<0x140>(x);
  return x;
}
__device__ __forceinline__ unsigned pack_bf16(float lo, float hi) {
  bf16x2 r; r[0] = (__bf16)lo; r[1] = (__bf16)hi;
  return __builtin_bit_cast(unsigned, r);
}
#define SB() __builtin_amdgcn_sched_barrier(0)

struct State {
  f32x4 acc[8];
  float m, l;
};

__device__ __forceinline__ void unit_scores(const u32x4 (&kk)[4], const u32x4 (&qv)[4], unsigned desc, int lq, float (&s)[4]) {
#pragma unroll
  for (int i = 0; i < 4; ++i) s[i] = 0.f;
#pragma unroll
  for (int b = 0; b < B; ++b) {
    if (desc & (1u << (28 + b))) {
      float x[4];
#pragma unroll
      for (int i = 0; i < 4; ++i) {
        float a = dot2(kk[i][0], qv[b][0], 0.f);
        a = dot2(kk[i][1], qv[b][1], a);
        a = dot2(kk[i][2], qv[b][2], a);
        a = dot2(kk[i][3], qv[b][3], a);
        x[i] = a;
      }
#pragma unroll
      for (int i = 0; i < 4; ++i) { x[i] = rowsum16(x[i]); s[i] = (lq == b) ? x[i] : s[i]; }
    }
  }
}

__device__ __forceinline__ void unit_softmax(State& st, float (&s)[4], unsigned desc, int lq, int ctxq, bool diag, u32x2& a) {
  const int j16 = (int)((desc >> 19) & 511u) * BS;
  const int lim = ((desc >> (28 + lq)) & 1u) ? ctxq - j16 : 0;
#pragma unroll
  for (int i = 0; i < 4; ++i) s[i] = (i < lim) ? s[i] : -INFINITY;
  const float mx = fmaxf(fmaxf(s[0], s[1]), fmaxf(s[2], s[3]));
  const float mn = fmaxf(st.m, mx * C2);
  const float alpha = __builtin_amdgcn_exp2f(st.m - mn);
  float p[4];
#pragma unroll
  for (int i = 0; i < 4; ++i) p[i] = __builtin_amdgcn_exp2f(fmaf(s[i], C2, -mn));
  st.l = fmaf(st.l, alpha, (p[0] + p[1]) + (p[2] + p[3]));
  const bool grew = mn > st.m;
  st.m = mn;
  a[0] = diag ? pack_bf16(p[0], p[1]) : 0u;
  a[1] = diag ? pack_bf16(p[2], p[3]) : 0u;
  if (__builtin_amdgcn_ballot_w64(grew) != 0) {
    const float a0 = dpp<0x00>(alpha), a1 = dpp<0x55>(alpha), a2 = dpp<0xAA>(alpha), a3 = dpp<0xFF>(alpha);
#pragma unroll
    for (int j = 0; j < 8; ++j) { st.acc[j][0] *= a0; st.acc[j][1] *= a1; st.acc[j][2] *= a2; st.acc[j][3] *= a3; }
  }
}

__device__ __forceinline__ void unit_vperm(const u32x4 (&vv)[4], u32x2 (&bop)[8]) {
#pragma unroll
  for (int d = 0; d < 4; ++d) {
    bop[2 * d][0]     = __builtin_amdgcn_perm(vv[1][d], vv[0][d], 0x05040100u);
    bop[2 * d][1]     = __builtin_amdgcn_perm(vv[3][d], vv[2][d], 0x05040100u);
    bop[2 * d + 1][0] = __builtin_amdgcn_perm(vv[1][d], vv[0][d], 0x07060302u);
    bop[2 * d + 1][1] = __builtin_amdgcn_perm(vv[3][d], vv[2][d], 0x07060302u);
  }
}
__device__ __forceinline__ void unit_pv(State& st, const u32x2& a, const u32x2 (&bop)[8]) {
#pragma unroll
  for (int j = 0; j < 8; ++j)
    st.acc[j] = __builtin_amdgcn_mfma_f32_16x16x16bf16_1k(__builtin_bit_cast(s16x4, a), __builtin_bit_cast(s16x4, bop[j]), st.acc[j], 0, 0, 0);
}

__global__ void __launch_bounds__(NTHR) attn_split(const unsigned short* __restrict__ q, const void* __restrict__ kc, const void* __restrict__ vc,
                                                   const int* __restrict__ bt, const int* __restrict__ ctx, float* __restrict__ po, float* __restrict__ pml, int nphys) {
  __shared__ __attribute__((aligned(16))) float lds[NW * 16 * D + NW * 32 + 64 + 16];
  float* lacc = lds;
  float* lml = lds + NW * 16 * D;
  unsigned* llist = (unsigned*)(lds + NW * 16 * D + NW * 32);
  unsigned* lcnt = llist + 64;
  const int tid = threadIdx.x, lane = tid & 63, w = __builtin_amdgcn_readfirstlane(tid >> 6);
  const int g = lane >> 4, c = lane & 15, lq = c & 3;
  const int hg = blockIdx.x / NSPLIT, s = blockIdx.x % NSPLIT;

  int cl[B];
#pragma unroll
  for (int b = 0; b < B; ++b) cl[b] = ctx[b];
  i32x2 tb[B];
#pragma unroll
  for (int b = 0; b < B; ++b) tb[b] = *(const i32x2*)(bt + b * NB + 2 * tid);
  u32x4 qv[B];
#pragma unroll
  for (int b = 0; b < B; ++b) qv[b] = *(const u32x4*)(q + ((b * H + hg * 4 + g) * D + 8 * c));
#pragma unroll
  for (int b = 0; b < B; ++b) {
    if (cl[b] <= 0) { cl[b] = NB * BS; qv[b] = u32x4{0u, 0u, 0u, 0u}; }
    if (cl[b] > NB * BS) cl[b] = NB * BS;
  }
  if (tid < 64) llist[tid] = (unsigned)nphys;
  unsigned msk[B][2]; int pre[B][2];
#pragma unroll
  for (int b = 0; b < B; ++b) {
    unsigned long long bal[2];
#pragma unroll
    for (int e = 0; e < 2; ++e) {
      const int j16 = (2 * tid + e) * BS;
      bool own = j16 < cl[b];
      unsigned mk = 1u << b;
#pragma unroll
      for (int o = 0; o < B; ++o) {
        if (o == b) continue;
        const bool same = (j16 < cl[o]) && (tb[o][e] == tb[b][e]);
        if (o < b) own = own && !same; else mk |= same ? (1u << o) : 0u;
      }
      msk[b][e] = own ? mk : 0u;
      bal[e] = __builtin_amdgcn_ballot_w64(own);
    }
    const int below = __builtin_amdgcn_mbcnt_hi((unsigned)(bal[0] >> 32), __builtin_amdgcn_mbcnt_lo((unsigned)bal[0], 0))
                    + __builtin_amdgcn_mbcnt_hi((unsigned)(bal[1] >> 32), __builtin_amdgcn_mbcnt_lo((unsigned)bal[1], 0));
    pre[b][0] = below; pre[b][1] = below + (msk[b][0] ? 1 : 0);
    if (lane == 0) lcnt[w * B + b] = (unsigned)(__builtin_popcountll(bal[0]) + __builtin_popcountll(bal[1]));
  }
  __syncthreads();
  int total = 0;
  {
    u32x4 cw[NW];
#pragma unroll
    for (int x = 0; x < NW; ++x) cw[x] = *(const u32x4*)(lcnt + x * B);
#pragma unroll
    for (int b = 0; b < B; ++b) {
      int base = total;
#pragma unroll
      for (int x = 0; x < NW; ++x) { base += (x < w) ? (int)cw[x][b] : 0; total += (int)cw[x][b]; }
#pragma unroll
      for (int e = 0; e < 2; ++e) {
        const int rank = base + pre[b][e];
        if (msk[b][e] && (rank % NSPLIT) == s) {
          const unsigned ph = min((unsigned)tb[b][e], (unsigned)nphys);
          llist[rank / NSPLIT] = ph | ((unsigned)(2 * tid + e) << 19) | (msk[b][e] << 28);
        }
      }
    }
  }
  __syncthreads();
  const unsigned descv = llist[lane];
  total = __builtin_amdgcn_readfirstlane(total);
  const int n = (total - s + NSPLIT - 1) / NSPLIT;

  const int ctxq = (lq == 0 ? cl[0] : lq == 1 ? cl[1] : lq == 2 ? cl[2] : cl[3]) - 4 * w;
  const bool diag = (c >> 2) == g;
  const auto rk = __builtin_amdgcn_make_buffer_rsrc((void*)kc, 0, nphys * BLKB, 0x00020000);
  const auto rv = __builtin_amdgcn_make_buffer_rsrc((void*)vc, 0, nphys * BLKB, 0x00020000);
  int voff[4];
#pragma unroll
  for (int i = 0; i < 4; ++i) voff[i] = (4 * w + i) * TOKB + (hg * 4 + g) * (D * 2) + c * 16;

  State st;
#pragma unroll
  for (int j = 0; j < 8; ++j) st.acc[j] = f32x4{0.f, 0.f, 0.f, 0.f};
  st.m = MINIT; st.l = 0.f;

  u32x4 kA[4], vA[4], kB[4], vB[4];
#define LOAD4(dst, rs, so) do { _Pragma("unroll") for (int i_ = 0; i_ < 4; ++i_) { dst[i_] = __builtin_amdgcn_raw_buffer_load_b128(rs, voff[i_], so, 2); SB(); } } while (0)
#define STEP(kc_, vc_, kn_, vn_, dcur, dnext) do { \
    const int so_ = (int)(((dnext) & 0x7FFFFu) * (unsigned)BLKB); \
    asm volatile("s_waitcnt vmcnt(4)" ::: "memory"); SB(); \
    LOAD4(kn_, rk, so_); \
    float sc_[4]; unit_scores(kc_, qv, dcur, lq, sc_); \
    SB(); asm volatile("s_waitcnt vmcnt(4)" ::: "memory"); SB(); \
    LOAD4(vn_, rv, so_); \
    u32x2 a_; unit_softmax(st, sc_, dcur, lq, ctxq, diag, a_); \
    u32x2 bop_[8]; unit_vperm(vc_, bop_); \
    unit_pv(st, a_, bop_); SB(); } while (0)
  unsigned d0 = __builtin_amdgcn_readlane(descv, 0);
  {
    const int so = (int)((d0 & 0x7FFFFu) * (unsigned)BLKB);
    LOAD4(kA, rk, so); LOAD4(vA, rv, so);
  }
  for (int u = 0; u < n; u += 2) {
    const unsigned d1 = (u + 1 < 64) ? __builtin_amdgcn_readlane(descv, (u + 1) & 63) : (unsigned)nphys;
    STEP(kA, vA, kB, vB, d0, d1);
    if (u + 1 >= n) break;
    d0 = (u + 2 < 64) ? __builtin_amdgcn_readlane(descv, (u + 2) & 63) : (unsigned)nphys;
    STEP(kB, vB, kA, vA, d1, d0);
  }

#pragma unroll
  for (int qq = 0; qq < B; ++qq) {
    float* dst = lacc + ((w * B + qq) * 4 + g) * D + 8 * c;
    *(f32x4*)dst = f32x4{st.acc[0][qq], st.acc[1][qq], st.acc[2][qq], st.acc[3][qq]};
    *(f32x4*)(dst + 4) = f32x4{st.acc[4][qq], st.acc[5][qq], st.acc[6][qq], st.acc[7][qq]};
  }
  if (c < 4) *(f32x2*)(lml + ((w * 4 + g) * 4 + c) * 2) = f32x2{st.m, st.l};
  __syncthreads();
  {
    const int qq = w, gg = g;
    f32x2 ml[NW];
#pragma unroll
    for (int x = 0; x < NW; ++x) ml[x] = *(const f32x2*)(lml + ((x * 4 + gg) * 4 + qq) * 2);
    float M = ml[0][0];
#pragma unroll
    for (int x = 1; x < NW; ++x) M = fmaxf(M, ml[x][0]);
    float L = 0.f; f32x4 o0 = {0.f, 0.f, 0.f, 0.f}, o1 = {0.f, 0.f, 0.f, 0.f};
#pragma unroll
    for (int x = 0; x < NW; ++x) {
      const float wt = __builtin_amdgcn_exp2f(ml[x][0] - M);
      const float* src = lacc + ((x * B + qq) * 4 + gg) * D + 8 * c;
      L = fmaf(wt, ml[x][1], L);
      o0 += wt * *(const f32x4*)src; o1 += wt * *(const f32x4*)(src + 4);
    }
    const int row = qq * H + hg * 4 + gg;
    float* dst = po + ((size_t)row * NSPLIT + s) * D + 8 * c;
    *(f32x4*)dst = o0; *(f32x4*)(dst + 4) = o1;
    if (c == 0) *(f32x2*)(pml + ((size_t)row * NSPLIT + s) * 2) = f32x2{M, L};
  }
}

__global__ void __launch_bounds__(256) attn_combine(const float* __restrict__ po, const float* __restrict__ pml, unsigned short* __restrict__ out) {
  __shared__ __attribute__((aligned(16))) float red[8 * D];
  const int row = blockIdx.x, tid = threadIdx.x, sg = tid >> 5, d4 = tid & 31;
  const f32x2 ml = *(const f32x2*)(pml + ((size_t)row * NSPLIT + d4) * 2);
  f32x4 v[4];
#pragma unroll
  for (int k = 0; k < 4; ++k) v[k] = *(const f32x4*)(po + ((size_t)row * NSPLIT + sg * 4 + k) * D + 4 * d4);
  float M = ml[0];
#pragma unroll
  for (int o = 16; o; o >>= 1) M = fmaxf(M, __shfl_xor(M, o));
  const float wt = __builtin_amdgcn_exp2f(ml[0] - M);
  float L = wt * ml[1];
#pragma unroll
  for (int o = 16; o; o >>= 1) L += __shfl_xor(L, o);
  f32x4 o4 = {0.f, 0.f, 0.f, 0.f};
#pragma unroll
  for (int k = 0; k < 4; ++k) o4 += __shfl(wt, sg * 4 + k, 32) * v[k];
  *(f32x4*)(red + sg * D + 4 * d4) = o4;
  __syncthreads();
  if (tid < 64) {
    float a = 0.f, b = 0.f;
#pragma unroll
    for (int x = 0; x < 8; ++x) { const f32x2 t = *(const f32x2*)(red + x * D + 2 * tid); a += t[0]; b += t[1]; }
    const float inv = 1.f / L;
    *(unsigned*)(out + row * D + 2 * tid) = pack_bf16(a * inv, b * inv);
  }
}
}

extern "C" void kernel_launch(void* const* d_in, const int* in_sizes, int n_in, void* d_out, int out_size, void* d_ws, size_t ws_size, hipStream_t stream) {
  const int nphys = in_sizes[1] / (BS * H * D);
  float* po = (float*)d_ws;
  float* pml = po + (size_t)B * H * NSPLIT * D;
  hipLaunchKernelGGL(attn_split, dim3(NWG), dim3(NTHR), 0, stream, (const unsigned short*)d_in[0], d_in[1], d_in[2], (const int*)d_in[3], (const int*)d_in[4], po, pml, nphys);
  hipLaunchKernelGGL(attn_combine, dim3(B * H), dim3(256), 0, stream, po, pml, (unsigned short*)d_out);
}
```

```cpp
#include <hip/hip_runtime.h>
#include <hip/hip_bf16.h>
#include <stdint.h>

namespace {
constexpr int B = 4, H = 32, D = 128, BS = 16, NB = 512;
constexpr int NHG = 8;
constexpr int NSPLIT = 32;
constexpr int NWG = NHG * NSPLIT;
constexpr int NS = 4;
constexpr int NW = NS + 4, NTHR = NW * 64;
constexpr int STRIDE = NSPLIT * NS;
constexpr int TOKB = H * D * 2;
constexpr int BLKB = BS * TOKB;
constexpr float C2 = 0.088388347648318440f * 1.4426950408889634f;
constexpr float MINIT = -1e30f;
#define STATIC_NUM 13
#define PUBTRIG 4
#define TMODE 0
#define PRETOUCH 0
#define STAUX 16
#define LDAUX 16
#define POLLSLEEP 0
#define POLLDELAY 30
constexpr int NPOOL = 4;
constexpr int IT0 = 3;
constexpr int WS_EPOCH = 0;
constexpr int WS_QUEUE = 4096;
constexpr int WS_ML = 65536;
#define MLREC 16
constexpr int WS_PO = 65536 + 128 * NSPLIT * 128;
constexpr int WS_END = WS_PO + 128 * NSPLIT * 512;
constexpr int OOB = 0x7FFFFF00;
constexpr unsigned HOLD = 1u << 26, ALIVE = 0xF0000000u | HOLD;
constexpr int SPIN_CAP = 1 << 22;

typedef unsigned u32x4 __attribute__((ext_vector_type(4)));
typedef unsigned u32x2 __attribute__((ext_vector_type(2)));
typedef int i32x2 __attribute__((ext_vector_type(2)));
typedef int i32x4 __attribute__((ext_vector_type(4)));
typedef float f32x4 __attribute__((ext_vector_type(4)));
typedef float f32x2 __attribute__((ext_vector_type(2)));
typedef short s16x4 __attribute__((ext_vector_type(4)));
typedef __bf16 bf16x2 __attribute__((ext_vector_type(2)));
typedef __bf16 bf16x8 __attribute__((ext_vector_type(8)));
#define GAS __attribute__((address_space(1)))
#define LAS __attribute__((address_space(3)))
#define CAS __attribute__((address_space(4)))

__device__ __forceinline__ float u2f(unsigned x) { return __builtin_bit_cast(float, x); }
__device__ __forceinline__ unsigned f2u(float x) { return __builtin_bit_cast(unsigned, x); }
template <int CTRL> __device__ __forceinline__ float dpp(float x) {
  return __builtin_bit_cast(float, __builtin_amdgcn_mov_dpp(__builtin_bit_cast(int, x), CTRL, 0xf, 0xf, true));
}
__device__ __forceinline__ float red16_max(float x) {
  x = fmaxf(x, dpp<0x124>(x));
  x = fmaxf(x, dpp<0x128>(x));
  auto s = __builtin_amdgcn_permlane16_swap(f2u(x), f2u(x), false, false);
  x = fmaxf(u2f(s[0]), u2f(s[1]));
  auto t = __builtin_amdgcn_permlane32_swap(f2u(x), f2u(x), false, false);
  return fmaxf(u2f(t[0]), u2f(t[1]));
}
__device__ __forceinline__ float red16_sum(float x) {
  x += dpp<0x124>(x);
  x += dpp<0x128>(x);
  auto s = __builtin_amdgcn_permlane16_swap(f2u(x), f2u(x), false, false);
  x = u2f(s[0]) + u2f(s[1]);
  auto t = __builtin_amdgcn_permlane32_swap(f2u(x), f2u(x), false, false);
  return u2f(t[0]) + u2f(t[1]);
}
__device__ __forceinline__ unsigned pack_bf16(float lo, float hi) {
  bf16x2 r; r[0] = (__bf16)lo; r[1] = (__bf16)hi;
  return __builtin_bit_cast(unsigned, r);
}
__device__ __forceinline__ u32x4 make_srd(const void* p, unsigned bytes) {
  const unsigned long long a = (unsigned long long)p;
  return u32x4{(unsigned)a, (unsigned)(a >> 32) & 0xFFFFu, bytes, 0x00020000u};
}
__device__ __forceinline__ u32x4 tag4(f32x4 v, unsigned tag) {
  return u32x4{__builtin_amdgcn_perm(f2u(v[0]), tag, 0x07060500u), __builtin_amdgcn_perm(f2u(v[1]), tag, 0x07060501u),
               __builtin_amdgcn_perm(f2u(v[2]), tag, 0x07060502u), __builtin_amdgcn_perm(f2u(v[3]), tag, 0x07060503u)};
}
__device__ __forceinline__ unsigned untag4(u32x4 v) {
  return __builtin_amdgcn_perm(__builtin_amdgcn_perm(v[3], v[2], 0x0c0c0400u), __builtin_amdgcn_perm(v[1], v[0], 0x0c0c0400u), 0x05040100u);
}
#define SB() __builtin_amdgcn_sched_barrier(0)
#define STAMP(i) do {} while (0)
#define LDS_BUMP(off) asm volatile("s_mov_b64 exec, 1\n\tds_add_u32 %0, %1\n\ts_mov_b64 exec, -1" :: "v"((unsigned)(uintptr_t)lds + (off)), "v"(1) : "memory")
#define SPIN_UNTIL(cond) _Pragma("nounroll") for (int spin_ = 0; spin_ < SPIN_CAP && !(cond); ++spin_) __builtin_amdgcn_s_sleep(1)

constexpr int RROW = 1088;
constexpr int HALF = 4 * RROW;
constexpr int SLOT = 2 * HALF;
constexpr int L_ARR = 0;
constexpr int L_CNT = 32;
constexpr int L_ML = L_CNT + 64;
constexpr int L_LIST = L_ML + B * 4 * NS * 8;
constexpr int L_DEP = L_LIST + (B * NB + 4) * 4;
constexpr int L_RING = L_DEP + B * NS * 4 * D * 4;
constexpr int L_END = L_RING + NS * 2 * SLOT;

__global__ void __launch_bounds__(NTHR, 2) attn_decode(const unsigned short* __restrict__ q, const void* __restrict__ kc, const void* __restrict__ vc,
                                                       const int* __restrict__ bt, const int* __restrict__ ctx, unsigned short* __restrict__ out,
                                                       unsigned char* __restrict__ ws, int nphys) {
  __shared__ __attribute__((aligned(16))) unsigned char lds_[L_END];
  LAS unsigned char* lds = (LAS unsigned char*)lds_;
  const int tid = threadIdx.x, lane = tid & 63, w = __builtin_amdgcn_readfirstlane(tid >> 6);
  const int g = lane >> 4, c = lane & 15, lq = c & 3;
  const int hg = (blockIdx.x >> 3) & 7, s = ((blockIdx.x & 7) << 2) | (blockIdx.x >> 6);
  volatile LAS unsigned* arr = (volatile LAS unsigned*)(lds + L_ARR);
  STAMP(0);
  asm volatile("" :: "s"(q), "s"(kc), "s"(vc), "s"(bt), "s"(ctx), "s"(out), "s"(ws), "s"(nphys));
  const unsigned empty = (unsigned)nphys << 2;

  if (__builtin_expect(w < NS, 1)) {
    i32x4 cl0 = *(const CAS i32x4*)ctx;
    int e00 = ((const CAS int*)bt)[s];
    u32x4 qf[4];
    int ev[B];
    asm volatile("global_load_dwordx4 %0, %8, %10\n\tglobal_load_dwordx4 %1, %8, %10 offset:64\n\t"
                 "global_load_dwordx4 %2, %8, %10 offset:128\n\tglobal_load_dwordx4 %3, %8, %10 offset:192\n\t"
                 "global_load_dword %4, %9, %11 offset:-4096\n\tglobal_load_dword %5, %9, %11 offset:-2048\n\t"
                 "global_load_dword %6, %9, %11\n\tglobal_load_dword %7, %9, %11 offset:2048"
                 : "=&v"(qf[0]), "=&v"(qf[1]), "=&v"(qf[2]), "=&v"(qf[3]), "=&v"(ev[0]), "=&v"(ev[1]), "=&v"(ev[2]), "=&v"(ev[3])
                 : "v"(((lq * H + hg * 4 + (c >> 2)) * D + 8 * g) * 2), "v"((2 * NB + s + NSPLIT * (lane & 7)) * 4), "s"(q), "s"(bt) : "memory");
    const u32x4 rk = make_srd(kc, (unsigned)nphys * BLKB), rv = make_srd(vc, (unsigned)nphys * BLKB), rw = make_srd(ws, WS_END);
    int voff[4];
#pragma unroll
    for (int i = 0; i < 4; ++i) voff[i] = i * (TOKB - RROW) + hg * 1024 + lane * 16;
    const unsigned ring = (unsigned)(uintptr_t)lds + L_RING + w * 2 * SLOT;
    asm volatile("" : "+v"(voff[0]), "+v"(voff[1]), "+v"(voff[2]), "+v"(voff[3]) : "s"(rk), "s"(rv), "s"(ring));
    asm volatile("" : "+s"(e00), "+s"(cl0));
#define DMA4(rs, so, dst) asm volatile("s_mov_b32 m0, %0\n\ts_nop 3\n\t" \
      "buffer_load_dwordx4 %1, %5, %6 offen nt lds\n\t" \
      "buffer_load_dwordx4 %2, %5, %6 offen offset:1088 nt lds\n\t" \
      "buffer_load_dwordx4 %3, %5, %6 offen offset:2176 nt lds\n\t" \
      "buffer_load_dwordx4 %4, %5, %6 offen offset:3264 nt lds" \
      :: "s"(dst), "v"(voff[0]), "v"(voff[1]), "v"(voff[2]), "v"(voff[3]), "s"(rs), "s"(so) : "memory")
    {
      const unsigned so = ((min((unsigned)e00, (unsigned)nphys) << 2) | (unsigned)w) << 15;
      DMA4(rk, so, ring);
      DMA4(rv, so, ring + HALF);
    }
    STAMP(1);
    __builtin_amdgcn_s_barrier();
    int cl[B];
#pragma unroll
    for (int b = 0; b < B; ++b) cl[b] = (cl0[b] <= 0 || cl0[b] > NB * BS) ? NB * BS : cl0[b];
    asm volatile("s_waitcnt vmcnt(8)" : "+v"(qf[0]), "+v"(qf[1]), "+v"(qf[2]), "+v"(qf[3]), "+v"(ev[0]), "+v"(ev[1]), "+v"(ev[2]), "+v"(ev[3]) :: "memory");
    unsigned descv;
    {
      const int j = s + NSPLIT * (lane & 7);
      unsigned mk = 1u;
#pragma unroll
      for (int b = 1; b < B; ++b) mk |= (j * BS < cl[b] && ev[b] == ev[0]) ? (1u << b) : 0u;
      descv = (unsigned)w | (min((unsigned)ev[0], (unsigned)nphys) << 2) | ((unsigned)j << 17) | (mk << 28);
      descv = j * BS < cl[0] ? descv : (empty | HOLD);
    }
    unsigned cur = __builtin_amdgcn_readlane(descv, 0);
    unsigned nxt = __builtin_amdgcn_readlane(descv, 1);
    int nidx = s * NS + w + ((cur & HOLD) ? 0 : STRIDE) + ((nxt & HOLD) ? 0 : STRIDE);
    const int cq0 = (lq & 2) ? ((lq & 1) ? cl0[3] : cl0[2]) : ((lq & 1) ? cl0[1] : cl0[0]);
#pragma unroll
    for (int x = 0; x < 4; ++x) if (cq0 <= 0) qf[x] = u32x4{0u, 0u, 0u, 0u};
    const int ctxq = ((c >> 2) != g) ? 0 : (cq0 <= 0 || cq0 > NB * BS) ? NB * BS : cq0;
    const int pool = (s + w) & (NPOOL - 1);
    const int qoff = lane == 0 ? WS_QUEUE + (hg * NPOOL + pool) * 256 : OOB;
    const int kaddr = (c & 3) * RROW + (c >> 2) * 256 + g * 16;
    const int vaddr = HALF + (c >> 2) * RROW + g * 256 + (c & 3) * 8;

    f32x4 acc[8];
#pragma unroll
    for (int j = 0; j < 8; ++j) acc[j] = f32x4{0.f, 0.f, 0.f, 0.f};
    float m = MINIT, l = 0.f;
    int qdone = 0, total = -1, u0 = 0x7FFFFFFF;
    unsigned sl = 0;

    do {
      const unsigned so = nxt << 15;
      const bool dyn = nidx >= u0;
      int tk = 1;
      asm volatile("s_waitcnt vmcnt(4)" ::: "memory");
      asm volatile("buffer_atomic_add %0, %1, %2, %3 offen sc0" : "+v"(tk) : "v"(qoff), "s"(rw), "s"((dyn && (nxt & ALIVE)) ? 0 : OOB) : "memory");
      DMA4(rk, so, ring + (sl ^ SLOT));
      const LAS unsigned char* sp = lds + L_RING + w * 2 * SLOT + sl;
      u32x4 kf[4];
#pragma unroll
      for (int x = 0; x < 4; ++x) kf[x] = *(const LAS u32x4*)(sp + kaddr + 64 * x);
      SB();
      asm volatile("s_waitcnt vmcnt(5)" ::: "memory");
      DMA4(rv, so, ring + (sl ^ SLOT) + HALF);
      SB();
      s16x4 bop[8];
#pragma unroll
      for (int j = 0; j < 8; ++j) bop[j] = __builtin_amdgcn_ds_read_tr16_b64_v4i16((LAS s16x4*)(sp + vaddr + 32 * j));
      f32x4 sc = {0.f, 0.f, 0.f, 0.f};
#pragma unroll
      for (int x = 0; x < 4; ++x)
        sc = __builtin_amdgcn_mfma_f32_16x16x32_bf16(__builtin_bit_cast(bf16x8, kf[x]), __builtin_bit_cast(bf16x8, qf[x]), sc, 0, 0, 0);
      const int tok0 = (int)((cur >> 17) & 511u) * BS + (int)(cur & 3u) * 4;
      int lim = ((cur >> (28 + lq)) & 1u) ? ctxq - tok0 : 0;
      asm volatile("" : "+v"(lim));
#pragma unroll
      for (int i = 0; i < 4; ++i) sc[i] = (i < lim) ? sc[i] : -INFINITY;
      const float mx = fmaxf(fmaxf(sc[0], sc[1]), fmaxf(sc[2], sc[3]));
      const float mn = fmaxf(m, mx * C2);
      float alpha = __builtin_amdgcn_exp2f(m - mn);
      float p[4];
#pragma unroll
      for (int i = 0; i < 4; ++i) p[i] = __builtin_amdgcn_exp2f(fmaf(sc[i], C2, -mn));
      l = fmaf(l, alpha, (p[0] + p[1]) + (p[2] + p[3]));
      const bool grew = mn > m;
      m = mn;
      const u32x2 a = {pack_bf16(p[0], p[1]), pack_bf16(p[2], p[3])};
      if (__builtin_amdgcn_ballot_w64(grew) != 0) {
        alpha = fminf(alpha, dpp<0x124>(alpha));
        alpha = fminf(alpha, dpp<0x128>(alpha));
        const float a0 = dpp<0x00>(alpha), a1 = dpp<0x55>(alpha), a2 = dpp<0xAA>(alpha), a3 = dpp<0xFF>(alpha);
#pragma unroll
        for (int j = 0; j < 8; ++j) { acc[j][0] *= a0; acc[j][1] *= a1; acc[j][2] *= a2; acc[j][3] *= a3; }
      }
#pragma unroll
      for (int j = 0; j < 8; ++j)
        acc[j] = __builtin_amdgcn_mfma_f32_16x16x16bf16_1k(__builtin_bit_cast(s16x4, a), bop[j], acc[j], 0, 0, 0);

      if (total < 0 && (nidx >= (IT0 + 2) * STRIDE || (nidx >> 2) * BS >= cl[0])) {
        SPIN_UNTIL(__builtin_amdgcn_readfirstlane(arr[4]) == 4u);
        total = __builtin_amdgcn_readfirstlane(arr[5]);
        u0 = max((IT0 + 3 + TMODE) * STRIDE, (total * 4 * STATIC_NUM / (16 * STRIDE)) * STRIDE);
        STAMP(6);
      }

      const int own_next = (nxt & HOLD) ? 0 : __builtin_ctz((nxt >> 28) | 16u);
      while (qdone < own_next) {
        const int qq = qdone++;
        int qsel = qq; asm volatile("" : "+v"(qsel));
        const bool b0 = qsel & 1, b1 = qsel & 2;
        LAS float* dst = (LAS float*)(lds + L_DEP) + ((qq * NS + w) * 4 + g) * D + c;
#pragma unroll
        for (int j = 0; j < 8; ++j) { const float lo = b0 ? acc[j][1] : acc[j][0], hi = b0 ? acc[j][3] : acc[j][2]; dst[16 * j] = b1 ? hi : lo; }
        if (c == 4 * g + qq) *(LAS f32x2*)(lds + L_ML + ((qq * 4 + g) * NS + w) * 8) = f32x2{m, l};
        LDS_BUMP(L_ARR + qq * 4);
      }

      asm volatile("s_waitcnt vmcnt(8)" : "+v"(tk) :: "memory");
      const int t = __builtin_amdgcn_readfirstlane(tk);
      const int un = dyn ? u0 + NPOOL * t + pool : nidx;
      const int r = (nxt & ALIVE) ? min(un >> 2, total) : total;
      cur = nxt;
      nxt = __builtin_amdgcn_readfirstlane(*(const LAS unsigned*)(lds + L_LIST + max(r, 0) * 4)) | (unsigned)(un & 3);
      if (total < 0) nxt = __builtin_amdgcn_readlane(descv, nidx / STRIDE);
      nidx += STRIDE;
      sl ^= SLOT;
    } while (cur & ALIVE);
    asm volatile("s_waitcnt vmcnt(0)" ::: "memory");
    STAMP(3);
    const auto rwb = __builtin_amdgcn_make_buffer_rsrc((void*)ws, 0, WS_END, 0x00020000);
    const int gg = s >> 3, slice = s & 7, pp = lane >> 2, e = lane & 3;
    const int row0 = ((hg * 4 + w) * 4 + gg) * NSPLIT;
    const int ao = WS_PO + (row0 + pp) * 512 + slice * 64 + e * 16, am = WS_ML + (row0 + pp) * MLREC;
    const unsigned tag = __builtin_amdgcn_readfirstlane(arr[7]);
    SPIN_UNTIL(__builtin_amdgcn_readfirstlane(arr[B - 1]) == (unsigned)NS);
    __builtin_amdgcn_s_sleep(POLLDELAY);
    u32x4 gv[2], gm[2];
    bool ok = false;
    _Pragma("nounroll") for (int spin = 0; spin < SPIN_CAP; ++spin) {
      if (!ok) {
#pragma unroll
        for (int i = 0; i < 2; ++i) {
          gv[i] = __builtin_amdgcn_raw_buffer_load_b128(rwb, ao + i * 16 * 512, 0, LDAUX);
          gm[i] = __builtin_amdgcn_raw_buffer_load_b128(rwb, am + i * 16 * MLREC, 0, LDAUX);
        }
        unsigned bad = 0;
#pragma unroll
        for (int i = 0; i < 2; ++i) bad |= (untag4(gv[i]) ^ tag) | (gm[i][1] ^ tag) | (gm[i][3] ^ tag);
        ok = bad == 0;
      }
      if (__builtin_amdgcn_ballot_w64(!ok) == 0) break;
      __builtin_amdgcn_s_sleep(POLLSLEEP);
    }
    STAMP(4);
    const float M = red16_max(fmaxf(u2f(gm[0][0]), u2f(gm[1][0])));
    const float w0 = __builtin_amdgcn_exp2f(u2f(gm[0][0]) - M), w1 = __builtin_amdgcn_exp2f(u2f(gm[1][0]) - M);
    const float L = red16_sum(w0 * u2f(gm[0][2]) + w1 * u2f(gm[1][2]));
    float o[4];
#pragma unroll
    for (int k = 0; k < 4; ++k) o[k] = red16_sum(w0 * u2f(gv[0][k] & 0xFFFFFF00u) + w1 * u2f(gv[1][k] & 0xFFFFFF00u));
    const float inv = 1.f / L;
    if (lane < 4)
      *(u32x2*)(out + (w * H + hg * 4 + gg) * D + slice * 16 + e * 4) = u32x2{pack_bf16(o[0] * inv, o[1] * inv), pack_bf16(o[2] * inv, o[3] * inv)};
    if (s == 0 && w == 0 && lane == 0) __hip_atomic_store((GAS unsigned*)(ws + WS_EPOCH + hg * 256), tag, __ATOMIC_RELAXED, __HIP_MEMORY_SCOPE_AGENT);
  } else {
    const int h = w - NS, tcol = (h * 64 + lane) * 2;
    const i32x4 cl0 = *(const CAS i32x4*)ctx;
    i32x2 tb[B];
#pragma unroll
    for (int b = 0; b < B; ++b) tb[b] = *(const i32x2*)(bt + b * NB + tcol);
    GAS unsigned* pe = (GAS unsigned*)(ws + WS_EPOCH + hg * 256);
    GAS unsigned long long* pq = (GAS unsigned long long*)(ws + WS_QUEUE + (hg * NPOOL + h) * 256);
    const unsigned ev = __hip_atomic_load(pe, __ATOMIC_RELAXED, __HIP_MEMORY_SCOPE_AGENT);
    unsigned long long wv = __hip_atomic_load(pq, __ATOMIC_RELAXED, __HIP_MEMORY_SCOPE_AGENT);
    if (h == 0 && lane < 8) arr[lane] = 0u;
    asm volatile("s_waitcnt lgkmcnt(0)" ::: "memory");
    SB();
    __builtin_amdgcn_s_barrier();
    SB();
    int cl[B];
#pragma unroll
    for (int b = 0; b < B; ++b) cl[b] = (cl0[b] <= 0 || cl0[b] > NB * BS) ? NB * BS : cl0[b];
    unsigned msk[B][2]; int pre[B];
    unsigned cntv = 0;
#pragma unroll
    for (int b = 0; b < B; ++b) {
      int below = 0, cnt = 0;
#pragma unroll
      for (int e = 0; e < 2; ++e) {
        const int j16 = (tcol + e) * BS;
        bool own = j16 < cl[b];
        unsigned mk = 1u << b;
#pragma unroll
        for (int o = 0; o < B; ++o) {
          if (o == b) continue;
          const bool same = (j16 < cl[o]) && (tb[o][e] == tb[b][e]);
          if (o < b) own = own && !same; else mk |= same ? (1u << o) : 0u;
        }
        msk[b][e] = own ? mk : 0u;
        const unsigned long long bal = __builtin_amdgcn_ballot_w64(own);
        below = __builtin_amdgcn_mbcnt_hi((unsigned)(bal >> 32), __builtin_amdgcn_mbcnt_lo((unsigned)bal, below));
        cnt += __builtin_popcountll(bal);
      }
      pre[b] = below;
      cntv = (lane == b) ? (unsigned)cnt : cntv;
    }
    if (lane < B) *(LAS unsigned*)(lds + L_CNT + (h * B + lane) * 4) = cntv;
    LDS_BUMP(L_ARR + 24);
    SPIN_UNTIL(__builtin_amdgcn_readfirstlane(arr[6]) == 4u);
    asm volatile("" ::: "memory");
    {
      int tot = 0;
      u32x4 cw[4];
#pragma unroll
      for (int x = 0; x < 4; ++x) cw[x] = *(const LAS u32x4*)(lds + L_CNT + x * 16);
#pragma unroll
      for (int b = 0; b < B; ++b) {
        int base = tot + pre[b];
#pragma unroll
        for (int x = 0; x < 4; ++x) { base += (x < h) ? (int)cw[x][b] : 0; tot += (int)cw[x][b]; }
#pragma unroll
        for (int e = 0; e < 2; ++e) {
          const unsigned ph = min((unsigned)tb[b][e], (unsigned)nphys);
          const int slot = msk[b][e] ? base : (B * NB + 1);
          *(LAS unsigned*)(lds + L_LIST + slot * 4) = (ph << 2) | ((unsigned)(tcol + e) << 17) | (msk[b][e] << 28);
          base += msk[b][e] ? 1 : 0;
        }
      }
      if (h == 0 && lane == 0) { *(LAS unsigned*)(lds + L_LIST + tot * 4) = empty; arr[5] = (unsigned)tot; }
    }
    const unsigned e32 = __builtin_amdgcn_readfirstlane(ev);
    const unsigned tag = ((e32 & 0x7F7F7F7Fu) + 0x01010101u) ^ (e32 & 0x80808080u);
    if (h == 0 && lane == 0) arr[7] = tag;
    if (lane == 0 && (unsigned)(wv >> 32) != tag)
      __hip_atomic_compare_exchange_strong(pq, &wv, (unsigned long long)tag << 32, __ATOMIC_RELAXED, __ATOMIC_RELAXED, __HIP_MEMORY_SCOPE_AGENT);
    asm volatile("s_waitcnt vmcnt(0)" ::: "memory");
    LDS_BUMP(L_ARR + 16);
    STAMP(1);

    const auto rw = __builtin_amdgcn_make_buffer_rsrc((void*)ws, 0, WS_END, 0x00020000);
    const int rec = ((hg * 4 + h) * 4 + g) * NSPLIT + s;
    const int po = WS_PO + rec * 512 + c * 16;
    SPIN_UNTIL(__builtin_amdgcn_readfirstlane(arr[h]) == (unsigned)NS);
    asm volatile("" ::: "memory");
    STAMP(2);
    {
      f32x2 ml[NS]; f32x4 d0[NS], d1[NS];
#pragma unroll
      for (int x = 0; x < NS; ++x) {
        ml[x] = *(const LAS f32x2*)(lds + L_ML + ((h * 4 + g) * NS + x) * 8);
        const LAS float* src = (const LAS float*)(lds + L_DEP) + ((h * NS + x) * 4 + g) * D + 4 * c;
        d0[x] = *(const LAS f32x4*)src; d1[x] = *(const LAS f32x4*)(src + 64);
      }
      float M = ml[0][0];
#pragma unroll
      for (int x = 1; x < NS; ++x) M = fmaxf(M, ml[x][0]);
      float L = 0.f;
      f32x4 o0 = {0.f, 0.f, 0.f, 0.f}, o1 = {0.f, 0.f, 0.f, 0.f};
#pragma unroll
      for (int x = 0; x < NS; ++x) {
        const float wt = __builtin_amdgcn_exp2f(ml[x][0] - M);
        L = fmaf(wt, ml[x][1], L);
        o0 += wt * d0[x]; o1 += wt * d1[x];
      }
      u32x4 t0 = tag4(o0, tag), t1 = tag4(o1, tag);
      asm volatile("" : "+v"(t0), "+v"(t1));
      SPIN_UNTIL(__builtin_amdgcn_readfirstlane(arr[B - 1]) >= (unsigned)PUBTRIG);
      __builtin_amdgcn_raw_buffer_store_b128(t0, rw, po, 0, STAUX);
      __builtin_amdgcn_raw_buffer_store_b128(t1, rw, po + 256, 0, STAUX);
      __builtin_amdgcn_raw_buffer_store_b128(u32x4{f2u(M), tag, f2u(L), tag}, rw, c < MLREC / 16 ? WS_ML + rec * MLREC + c * 16 : OOB, 0, STAUX);
    }
    STAMP(3);
  }
}
}

extern "C" void kernel_launch(void* const* d_in, const int* in_sizes, int n_in, void* d_out, int out_size, void* d_ws, size_t ws_size, hipStream_t stream) {
  const int nphys = in_sizes[1] / (BS * H * D);
  hipLaunchKernelGGL(attn_decode, dim3(NWG), dim3(NTHR), 0, stream, (const unsigned short*)d_in[0], d_in[1], d_in[2], (const int*)d_in[3], (const int*)d_in[4],
                     (unsigned short*)d_out, (unsigned char*)d_ws, nphys);
}
```

```cpp
#include <hip/hip_runtime.h>
#include <hip/hip_bf16.h>
#include <stdint.h>

namespace {
constexpr int B = 4, H = 32, D = 128, BS = 16, NB = 512;
constexpr int NHG = 8;
constexpr int NSPLIT = 32;
constexpr int NWG = NHG * NSPLIT;
constexpr int NS = 4;
constexpr int NW = NS + 4, NTHR = NW * 64;
constexpr int TOKB = H * D * 2;
constexpr int BLKB = BS * TOKB;
constexpr float C2 = 0.088388347648318440f * 1.4426950408889634f;
constexpr float MINIT = -1e30f;
#define POLLDELAY 30
#define PACE 871
#define PACE0 0
#define LEAD 3
#define WAITFIRST 1
constexpr int EARLY = 13;
constexpr int WS_EPOCH = 0;
constexpr int WS_ML = 65536;
constexpr int WS_PO = 131072;
constexpr int WS_END = WS_PO + 128 * NSPLIT * 512;
constexpr int OOB = 0x7FFFFF00;
constexpr int SPIN_CAP = 1 << 22;

typedef unsigned u32x4 __attribute__((ext_vector_type(4)));
typedef unsigned u32x2 __attribute__((ext_vector_type(2)));
typedef int i32x2 __attribute__((ext_vector_type(2)));
typedef int i32x4 __attribute__((ext_vector_type(4)));
typedef float f32x4 __attribute__((ext_vector_type(4)));
typedef float f32x2 __attribute__((ext_vector_type(2)));
typedef short s16x4 __attribute__((ext_vector_type(4)));
typedef __bf16 bf16x2 __attribute__((ext_vector_type(2)));
typedef __bf16 bf16x8 __attribute__((ext_vector_type(8)));
#define GAS __attribute__((address_space(1)))
#define LAS __attribute__((address_space(3)))
#define CAS __attribute__((address_space(4)))

__device__ __forceinline__ float u2f(unsigned x) { return __builtin_bit_cast(float, x); }
__device__ __forceinline__ unsigned f2u(float x) { return __builtin_bit_cast(unsigned, x); }
template <int CTRL> __device__ __forceinline__ float dpp(float x) {
  return __builtin_bit_cast(float, __builtin_amdgcn_mov_dpp(__builtin_bit_cast(int, x), CTRL, 0xf, 0xf, true));
}
__device__ __forceinline__ float red16_max(float x) {
  x = fmaxf(x, dpp<0x124>(x));
  x = fmaxf(x, dpp<0x128>(x));
  auto s = __builtin_amdgcn_permlane16_swap(f2u(x), f2u(x), false, false);
  x = fmaxf(u2f(s[0]), u2f(s[1]));
  auto t = __builtin_amdgcn_permlane32_swap(f2u(x), f2u(x), false, false);
  return fmaxf(u2f(t[0]), u2f(t[1]));
}
__device__ __forceinline__ float red16_sum(float x) {
  x += dpp<0x124>(x);
  x += dpp<0x128>(x);
  auto s = __builtin_amdgcn_permlane16_swap(f2u(x), f2u(x), false, false);
  x = u2f(s[0]) + u2f(s[1]);
  auto t = __builtin_amdgcn_permlane32_swap(f2u(x), f2u(x), false, false);
  return u2f(t[0]) + u2f(t[1]);
}
__device__ __forceinline__ unsigned pack_bf16(float lo, float hi) {
  bf16x2 r; r[0] = (__bf16)lo; r[1] = (__bf16)hi;
  return __builtin_bit_cast(unsigned, r);
}
__device__ __forceinline__ u32x4 make_srd(const void* p, unsigned bytes) {
  const unsigned long long a = (unsigned long long)p;
  return u32x4{(unsigned)a, (unsigned)(a >> 32) & 0xFFFFu, bytes, 0x00020000u};
}
__device__ __forceinline__ u32x4 tag4(f32x4 v, unsigned tag) {
  return u32x4{__builtin_amdgcn_perm(f2u(v[0]), tag, 0x07060500u), __builtin_amdgcn_perm(f2u(v[1]), tag, 0x07060501u),
               __builtin_amdgcn_perm(f2u(v[2]), tag, 0x07060502u), __builtin_amdgcn_perm(f2u(v[3]), tag, 0x07060503u)};
}
__device__ __forceinline__ unsigned untag4(u32x4 v) {
  return __builtin_amdgcn_perm(__builtin_amdgcn_perm(v[3], v[2], 0x0c0c0400u), __builtin_amdgcn_perm(v[1], v[0], 0x0c0c0400u), 0x05040100u);
}
#define SB() __builtin_amdgcn_sched_barrier(0)
#define STAMP(i) do {} while (0)
#define USTAMP(v, i) do {} while (0)
#define LDS_BUMP(off) asm volatile("s_mov_b64 exec, 1\n\tds_add_u32 %0, %1\n\ts_mov_b64 exec, -1" :: "v"((unsigned)(uintptr_t)lds + (off)), "v"(1) : "memory")
#define PACE_WAIT() do { tsched += pace; _Pragma("nounroll") for (int z_ = 0; z_ < 64 && (int)(((unsigned)__builtin_amdgcn_s_memrealtime() << 4) - tsched) < 0; ++z_) __builtin_amdgcn_s_sleep(1); } while (0)
#define SPIN_UNTIL(cond) _Pragma("nounroll") for (int spin_ = 0; spin_ < SPIN_CAP && !(cond); ++spin_) __builtin_amdgcn_s_sleep(1)

constexpr int RROW = 1088;
constexpr int HALF = 4 * RROW;
constexpr int SCW = 64 * 256 + 1024;
constexpr int L_ARR = 0;
constexpr int L_CNT = 32;
constexpr int L_ML = L_CNT + 64;
constexpr int L_LIST = L_ML + B * 4 * NS * 8;
constexpr int L_SC = L_LIST + 68 * 4;
constexpr int L_RING = L_SC + NS * SCW;
constexpr int L_END = L_RING + NS * 4 * HALF;

__global__ void __launch_bounds__(NTHR, 2) attn_decode(const unsigned short* __restrict__ q, const void* __restrict__ kc, const void* __restrict__ vc,
                                                       const int* __restrict__ bt, const int* __restrict__ ctx, unsigned short* __restrict__ out,
                                                       unsigned char* __restrict__ ws, int nphys) {
  __shared__ __attribute__((aligned(16))) unsigned char lds_[L_END];
  LAS unsigned char* lds = (LAS unsigned char*)lds_;
  const int tid = threadIdx.x, lane = tid & 63, w = __builtin_amdgcn_readfirstlane(tid >> 6);
  const int g = lane >> 4, c = lane & 15, lq = c & 3;
  const int hg = (blockIdx.x >> 3) & 7, s = ((blockIdx.x & 7) << 2) | (blockIdx.x >> 6);
  volatile LAS unsigned* arr = (volatile LAS unsigned*)(lds + L_ARR);
  STAMP(0);
  asm volatile("" :: "s"(q), "s"(kc), "s"(vc), "s"(bt), "s"(ctx), "s"(out), "s"(ws), "s"(nphys));
  const unsigned empty = (unsigned)nphys << 2;

  if (__builtin_expect(w < NS, 1)) {
    i32x4 cl0 = *(const CAS i32x4*)ctx;
    int e00 = ((const CAS int*)bt)[s], e01 = ((const CAS int*)bt)[s + NSPLIT], e02 = ((const CAS int*)bt)[s + 2 * NSPLIT];
    u32x4 qf[4];
    int ev[B];
    asm volatile("global_load_dwordx4 %0, %8, %10\n\tglobal_load_dwordx4 %1, %8, %10 offset:64\n\t"
                 "global_load_dwordx4 %2, %8, %10 offset:128\n\tglobal_load_dwordx4 %3, %8, %10 offset:192\n\t"
                 "global_load_dword %4, %9, %11 offset:-4096\n\tglobal_load_dword %5, %9, %11 offset:-2048\n\t"
                 "global_load_dword %6, %9, %11\n\tglobal_load_dword %7, %9, %11 offset:2048"
                 : "=&v"(qf[0]), "=&v"(qf[1]), "=&v"(qf[2]), "=&v"(qf[3]), "=&v"(ev[0]), "=&v"(ev[1]), "=&v"(ev[2]), "=&v"(ev[3])
                 : "v"(((lq * H + hg * 4 + (c >> 2)) * D + 8 * g) * 2), "v"((2 * NB + s + NSPLIT * c) * 4), "s"(q), "s"(bt) : "memory");
    const u32x4 rk = make_srd(kc, (unsigned)nphys * BLKB), rv = make_srd(vc, (unsigned)nphys * BLKB);
    int voff[4];
#pragma unroll
    for (int i = 0; i < 4; ++i) voff[i] = i * (TOKB - RROW) + hg * 1024 + lane * 16;
    const unsigned ring = (unsigned)(uintptr_t)lds + L_RING + w * 4 * HALF;
    asm volatile("" : "+v"(voff[0]), "+v"(voff[1]), "+v"(voff[2]), "+v"(voff[3]) : "s"(rk), "s"(ring));
    asm volatile("" : "+s"(e00), "+s"(e01), "+s"(e02), "+s"(cl0));
#define DMA4(rs, so, dst) asm volatile("s_mov_b32 m0, %0\n\ts_nop 3\n\t" \
      "buffer_load_dwordx4 %1, %5, %6 offen nt lds\n\t" \
      "buffer_load_dwordx4 %2, %5, %6 offen offset:1088 nt lds\n\t" \
      "buffer_load_dwordx4 %3, %5, %6 offen offset:2176 nt lds\n\t" \
      "buffer_load_dwordx4 %4, %5, %6 offen offset:3264 nt lds" \
      :: "s"(dst), "v"(voff[0]), "v"(voff[1]), "v"(voff[2]), "v"(voff[3]), "s"(rs), "s"(so) : "memory")
    DMA4(rk, ((min((unsigned)e00, (unsigned)nphys) << 2) | (unsigned)w) << 15, ring);
    DMA4(rk, ((min((unsigned)e01, (unsigned)nphys) << 2) | (unsigned)w) << 15, ring + HALF);
    DMA4(rk, ((min((unsigned)e02, (unsigned)nphys) << 2) | (unsigned)w) << 15, ring + 2 * HALF);
#define QWAIT "12"
#define WPRE "8"
#define WPOST "12"
    STAMP(1);
    unsigned tsched = ((unsigned)__builtin_amdgcn_s_memrealtime() << 4) + (LEAD - 1) * PACE + PACE0, pace = PACE;
    __builtin_amdgcn_s_barrier();
    int cl[B];
#pragma unroll
    for (int b = 0; b < B; ++b) cl[b] = (cl0[b] <= 0 || cl0[b] > NB * BS) ? NB * BS : cl0[b];
    asm volatile("s_waitcnt vmcnt(" QWAIT ")" : "+v"(qf[0]), "+v"(qf[1]), "+v"(qf[2]), "+v"(qf[3]), "+v"(ev[0]), "+v"(ev[1]), "+v"(ev[2]), "+v"(ev[3]) :: "memory");
    unsigned descv;
    {
      const int j = s + NSPLIT * c;
      unsigned mk = 1u;
#pragma unroll
      for (int b = 1; b < B; ++b) mk |= (j * BS < cl[b] && ev[b] == ev[0]) ? (1u << b) : 0u;
      descv = (unsigned)w | (min((unsigned)ev[0], (unsigned)nphys) << 2) | ((unsigned)j << 17) | (mk << 28);
      descv = j * BS < cl[0] ? descv : empty;
    }
    const int cq0 = (lq & 2) ? ((lq & 1) ? cl0[3] : cl0[2]) : ((lq & 1) ? cl0[1] : cl0[0]);
#pragma unroll
    for (int x = 0; x < 4; ++x) if (cq0 <= 0) qf[x] = u32x4{0u, 0u, 0u, 0u};
    const bool diag = (c >> 2) == g;
    const int ctxq = !diag ? 0 : (cq0 <= 0 || cq0 > NB * BS) ? NB * BS : cq0;
    const LAS unsigned char* kp = lds + L_RING + w * 4 * HALF + (c & 3) * RROW + (c >> 2) * 256 + g * 16;
    const LAS unsigned char* vp = lds + L_RING + w * 4 * HALF + (c >> 2) * RROW + g * 256 + (c & 3) * 8;
    LAS unsigned char* scp = lds + L_SC + w * SCW + (diag ? c * 16 : 64 * 256 + lane * 16);
    const int scs = diag ? 256 : 0;

    f32x4 acc[8];
#pragma unroll
    for (int j = 0; j < 8; ++j) acc[j] = f32x4{0.f, 0.f, 0.f, 0.f};
    float m = MINIT, l = 0.f;
    int n = 0x10000;

    auto scores = [&](int slot, unsigned d) -> f32x4 {
      u32x4 kf[4];
#pragma unroll
      for (int x = 0; x < 4; ++x) kf[x] = *(const LAS u32x4*)(kp + slot * HALF + 64 * x);
      f32x4 sc = {0.f, 0.f, 0.f, 0.f};
#pragma unroll
      for (int x = 0; x < 4; ++x)
        sc = __builtin_amdgcn_mfma_f32_16x16x32_bf16(__builtin_bit_cast(bf16x8, kf[x]), __builtin_bit_cast(bf16x8, qf[x]), sc, 0, 0, 0);
      const int tok0 = (int)((d >> 17) & 511u) * BS + (int)(d & 3u) * 4;
      int lim = ((d >> (28 + lq)) & 1u) ? ctxq - tok0 : 0;
      asm volatile("" : "+v"(lim));
#pragma unroll
      for (int i = 0; i < 4; ++i) sc[i] = (i < lim) ? sc[i] : -INFINITY;
      return sc;
    };
    auto need_list = [&](int pos) {
      if (n == 0x10000 && (pos >= EARLY || (__builtin_amdgcn_readlane(descv, pos) >> 28) == 0u)) {
        SPIN_UNTIL(__builtin_amdgcn_readfirstlane(arr[1]) == 4u);
        const int total = __builtin_amdgcn_readfirstlane(arr[2]);
        int skip = 0;
#pragma unroll
        for (int k = 0; k < LEAD; ++k) skip += (__builtin_amdgcn_readlane(descv, k) >> 28) == 0u;
        const int mine = (total - s + NSPLIT - 1) / NSPLIT;
        const unsigned d = *(const LAS unsigned*)(lds + L_LIST + max(lane - skip, 0) * 4) | (unsigned)w;
        descv = lane < LEAD ? descv : (lane - skip < mine) ? d : empty;
        n = max(pos, mine + skip);
        pace = (unsigned)(PACE * total) / (unsigned)(NSPLIT * n);
        STAMP(6);
      }
    };

    need_list(LEAD);
    for (int i = 0; i < n; ++i) {
      const bool isv = i + LEAD >= n;
      const unsigned so = (unsigned)__builtin_amdgcn_readlane(descv, isv ? i + LEAD - n : i + LEAD) << 15;
      const u32x4 rs = {isv ? rv[0] : rk[0], isv ? rv[1] : rk[1], rk[2], rk[3]};
      PACE_WAIT();
      if (WAITFIRST) asm volatile("s_waitcnt vmcnt(" WPRE ")" ::: "memory");
      DMA4(rs, so, ring + ((i + LEAD) & 3) * HALF);
      if (!WAITFIRST) asm volatile("s_waitcnt vmcnt(" WPOST ")" ::: "memory");
      const f32x4 sc = scores(i & 3, __builtin_amdgcn_readlane(descv, i));
      m = fmaxf(m, fmaxf(fmaxf(sc[0], sc[1]), fmaxf(sc[2], sc[3])) * C2);
      *(LAS f32x4*)(scp + i * scs) = sc;
      need_list(i + LEAD + 1);
    }
    STAMP(10);
    for (int i = 0; i < n; ++i) {
      const bool more = i + LEAD < n;
      const unsigned so = (more ? (unsigned)__builtin_amdgcn_readlane(descv, (i + LEAD) & 63) : empty) << 15;
      const f32x4 sc = *(const LAS f32x4*)(scp + i * scs);
      float p[4];
#pragma unroll
      for (int k = 0; k < 4; ++k) p[k] = __builtin_amdgcn_exp2f(fmaf(sc[k], C2, -m));
      l += (p[0] + p[1]) + (p[2] + p[3]);
      u32x2 a = {pack_bf16(p[0], p[1]), pack_bf16(p[2], p[3])};
      asm volatile("" : "+v"(a));
      if (more) PACE_WAIT();
      if (WAITFIRST) asm volatile("s_waitcnt vmcnt(" WPRE ")" ::: "memory");
      DMA4(rv, so, ring + ((n + i + LEAD) & 3) * HALF);
      if (!WAITFIRST) asm volatile("s_waitcnt vmcnt(" WPOST ")" ::: "memory");
      const LAS unsigned char* sp = vp + ((n + i) & 3) * HALF;
#pragma unroll
      for (int j = 0; j < 8; ++j)
        acc[j] = __builtin_amdgcn_mfma_f32_16x16x16bf16_1k(__builtin_bit_cast(s16x4, a), __builtin_amdgcn_ds_read_tr16_b64_v4i16((LAS s16x4*)(sp + 32 * j)), acc[j], 0, 0, 0);
    }
    STAMP(11);

    {
      LAS float* dst = (LAS float*)(lds + L_SC + w * SCW) + g * D + c;
#pragma unroll
      for (int qq = 0; qq < B; ++qq)
#pragma unroll
        for (int j = 0; j < 8; ++j) dst[qq * 4 * D + 16 * j] = acc[j][qq];
      if (diag) *(LAS f32x2*)(lds + L_ML + ((lq * 4 + g) * NS + w) * 8) = f32x2{m, l};
      LDS_BUMP(L_ARR);
    }
    asm volatile("s_waitcnt vmcnt(0)" ::: "memory");
    STAMP(3);
    const auto rwb = __builtin_amdgcn_make_buffer_rsrc((void*)ws, 0, WS_END, 0x00020000);
    const int gg = s >> 3, slice = s & 7, pp = lane >> 2;
    const int row0 = ((hg * 4 + w) * 4 + gg) * NSPLIT;
    const int ao = WS_PO + (row0 + pp) * 512 + slice * 64 + lq * 16, am = WS_ML + (row0 + pp) * 16;
    const unsigned tag = __builtin_amdgcn_readfirstlane(arr[4]);
    SPIN_UNTIL(__builtin_amdgcn_readfirstlane(arr[0]) == (unsigned)NS);
    __builtin_amdgcn_s_sleep(POLLDELAY);
    u32x4 gv[2], gm[2];
    bool ok = false;
    _Pragma("nounroll") for (int spin = 0; spin < SPIN_CAP; ++spin) {
      if (!ok) {
#pragma unroll
        for (int i = 0; i < 2; ++i) {
          gv[i] = __builtin_amdgcn_raw_buffer_load_b128(rwb, ao + i * 16 * 512, 0, 16);
          gm[i] = __builtin_amdgcn_raw_buffer_load_b128(rwb, am + i * 16 * 16, 0, 16);
        }
        unsigned bad = 0;
#pragma unroll
        for (int i = 0; i < 2; ++i) bad |= (untag4(gv[i]) ^ tag) | (gm[i][1] ^ tag) | (gm[i][3] ^ tag);
        ok = bad == 0;
      }
      if (__builtin_amdgcn_ballot_w64(!ok) == 0) break;
    }
    STAMP(4);
    const float M = red16_max(fmaxf(u2f(gm[0][0]), u2f(gm[1][0])));
    const float w0 = __builtin_amdgcn_exp2f(u2f(gm[0][0]) - M), w1 = __builtin_amdgcn_exp2f(u2f(gm[1][0]) - M);
    const float L = red16_sum(w0 * u2f(gm[0][2]) + w1 * u2f(gm[1][2]));
    float o[4];
#pragma unroll
    for (int k = 0; k < 4; ++k) o[k] = red16_sum(w0 * u2f(gv[0][k] & 0xFFFFFF00u) + w1 * u2f(gv[1][k] & 0xFFFFFF00u));
    const float inv = 1.f / L;
    if (lane < 4)
      *(u32x2*)(out + (w * H + hg * 4 + gg) * D + slice * 16 + lane * 4) = u32x2{pack_bf16(o[0] * inv, o[1] * inv), pack_bf16(o[2] * inv, o[3] * inv)};
    if (s == 0 && w == 0 && lane == 0)
      __hip_atomic_store((GAS unsigned*)(ws + WS_EPOCH + hg * 256), tag, __ATOMIC_RELAXED, __HIP_MEMORY_SCOPE_AGENT);
  } else {
    const int h = w - NS, tcol = (h * 64 + lane) * 2;
    const i32x4 cl0 = *(const CAS i32x4*)ctx;
    i32x2 tb[B];
#pragma unroll
    for (int b = 0; b < B; ++b) tb[b] = *(const i32x2*)(bt + b * NB + tcol);
    GAS unsigned* pe = (GAS unsigned*)(ws + WS_EPOCH + hg * 256);
    const unsigned ev = __hip_atomic_load(pe, __ATOMIC_RELAXED, __HIP_MEMORY_SCOPE_AGENT);
    if (h == 0 && lane < 8) arr[lane] = 0u;
    asm volatile("s_waitcnt lgkmcnt(0)" ::: "memory");
    SB();
    __builtin_amdgcn_s_barrier();
    SB();
    int cl[B];
#pragma unroll
    for (int b = 0; b < B; ++b) cl[b] = (cl0[b] <= 0 || cl0[b] > NB * BS) ? NB * BS : cl0[b];
    unsigned msk[B][2]; int pre[B];
    unsigned cntv = 0;
#pragma unroll
    for (int b = 0; b < B; ++b) {
      int below = 0, cnt = 0;
#pragma unroll
      for (int e = 0; e < 2; ++e) {
        const int j16 = (tcol + e) * BS;
        bool own = j16 < cl[b];
        unsigned mk = 1u << b;
#pragma unroll
        for (int o = 0; o < B; ++o) {
          if (o == b) continue;
          const bool same = (j16 < cl[o]) && (tb[o][e] == tb[b][e]);
          if (o < b) own = own && !same; else mk |= same ? (1u << o) : 0u;
        }
        msk[b][e] = own ? mk : 0u;
        const unsigned long long bal = __builtin_amdgcn_ballot_w64(own);
        below = __builtin_amdgcn_mbcnt_hi((unsigned)(bal >> 32), __builtin_amdgcn_mbcnt_lo((unsigned)bal, below));
        cnt += __builtin_popcountll(bal);
      }
      pre[b] = below;
      cntv = (lane == b) ? (unsigned)cnt : cntv;
    }
    if (lane < B) *(LAS unsigned*)(lds + L_CNT + (h * B + lane) * 4) = cntv;
    LDS_BUMP(L_ARR + 12);
    SPIN_UNTIL(__builtin_amdgcn_readfirstlane(arr[3]) == 4u);
    asm volatile("" ::: "memory");
    {
      int tot = 0;
      u32x4 cw[4];
#pragma unroll
      for (int x = 0; x < 4; ++x) cw[x] = *(const LAS u32x4*)(lds + L_CNT + x * 16);
#pragma unroll
      for (int b = 0; b < B; ++b) {
        int base = tot + pre[b];
#pragma unroll
        for (int x = 0; x < 4; ++x) { base += (x < h) ? (int)cw[x][b] : 0; tot += (int)cw[x][b]; }
#pragma unroll
        for (int e = 0; e < 2; ++e) {
          const unsigned ph = min((unsigned)tb[b][e], (unsigned)nphys);
          const int slot = (msk[b][e] && (base & (NSPLIT - 1)) == s) ? base / NSPLIT : 64;
          *(LAS unsigned*)(lds + L_LIST + slot * 4) = (ph << 2) | ((unsigned)(tcol + e) << 17) | (msk[b][e] << 28);
          base += msk[b][e] ? 1 : 0;
        }
      }
      if (h == 0 && lane == 0) arr[2] = (unsigned)tot;
    }
    const unsigned e32 = __builtin_amdgcn_readfirstlane(ev);
    const unsigned tag = ((e32 & 0x7F7F7F7Fu) + 0x01010101u) ^ (e32 & 0x80808080u);
    if (h == 0 && lane == 0) arr[4] = tag;
    asm volatile("" :: "s"(tag) : "memory");
    LDS_BUMP(L_ARR + 4);
    STAMP(1);

    const auto rw = __builtin_amdgcn_make_buffer_rsrc((void*)ws, 0, WS_END, 0x00020000);
    const int rec = ((hg * 4 + h) * 4 + g) * NSPLIT + s;
    const int po = WS_PO + rec * 512 + c * 16;
    SPIN_UNTIL(__builtin_amdgcn_readfirstlane(arr[0]) == (unsigned)NS);
    asm volatile("" ::: "memory");
    STAMP(2);
    f32x2 ml[NS]; f32x4 d0[NS], d1[NS];
#pragma unroll
    for (int x = 0; x < NS; ++x) {
      ml[x] = *(const LAS f32x2*)(lds + L_ML + ((h * 4 + g) * NS + x) * 8);
      const LAS float* src = (const LAS float*)(lds + L_SC + x * SCW) + (h * 4 + g) * D + 4 * c;
      d0[x] = *(const LAS f32x4*)src; d1[x] = *(const LAS f32x4*)(src + 64);
    }
    float M = ml[0][0];
#pragma unroll
    for (int x = 1; x < NS; ++x) M = fmaxf(M, ml[x][0]);
    float L = 0.f;
    f32x4 o0 = {0.f, 0.f, 0.f, 0.f}, o1 = {0.f, 0.f, 0.f, 0.f};
#pragma unroll
    for (int x = 0; x < NS; ++x) {
      const float wt = __builtin_amdgcn_exp2f(ml[x][0] - M);
      L = fmaf(wt, ml[x][1], L);
      o0 += wt * d0[x]; o1 += wt * d1[x];
    }
    __builtin_amdgcn_raw_buffer_store_b128(tag4(o0, tag), rw, po, 0, 16);
    __builtin_amdgcn_raw_buffer_store_b128(tag4(o1, tag), rw, po + 256, 0, 16);
    __builtin_amdgcn_raw_buffer_store_b128(u32x4{f2u(M), tag, f2u(L), tag}, rw, c == 0 ? WS_ML + rec * 16 : OOB, 0, 16);
    STAMP(3);
  }
}
}

extern "C" void kernel_launch(void* const* d_in, const int* in_sizes, int n_in, void* d_out, int out_size, void* d_ws, size_t ws_size, hipStream_t stream) {
  const int nphys = in_sizes[1] / (BS * H * D);
  hipLaunchKernelGGL(attn_decode, dim3(NWG), dim3(NTHR), 0, stream, (const unsigned short*)d_in[0], d_in[1], d_in[2], (const int*)d_in[3], (const int*)d_in[4],
                     (unsigned short*)d_out, (unsigned char*)d_ws, nphys);
}
```

```cpp
#include <hip/hip_runtime.h>
#include <hip/hip_bf16.h>
#include <stdint.h>

namespace {
constexpr int B = 4, H = 32, D = 128, BS = 16, NB = 512;
constexpr int NHG = 8;
constexpr int NSPLIT = 32;
constexpr int NWG = NHG * NSPLIT;
constexpr int NS = 4;
constexpr int NW = NS + 4, NTHR = NW * 64;
constexpr int TOKB = H * D * 2;
constexpr int BLKB = BS * TOKB;
constexpr float C2 = 0.088388347648318440f * 1.4426950408889634f;
constexpr float MINIT = -1e30f;
#define POLLDELAY 27
#define PACE 860
#define PACE0 1200
#define CTRL 1
#define VMTHR 4
#define STRETCH 40
#define REPAY 20
#define XLATE 400
#define COMMON 1
#define CODEPF 8192
#define STAG 1
#define LEAD 3
#define HSLEEP 0
#define PUBLEAD 1
#define MEANAT 7000
#define SPRINT 0
#define TOUCH 0
constexpr int EARLY = 13;
constexpr int WS_EPOCH = 0;
constexpr int WS_START = 4096;
constexpr int WS_ML = 65536;
constexpr int WS_PO = 131072;
constexpr int WS_END = WS_PO + 128 * NSPLIT * 512;
constexpr int OOB = 0x7FFFFF00;
constexpr int SPIN_CAP = 1 << 22;

typedef unsigned u32x4 __attribute__((ext_vector_type(4)));
typedef unsigned u32x2 __attribute__((ext_vector_type(2)));
typedef int i32x2 __attribute__((ext_vector_type(2)));
typedef int i32x4 __attribute__((ext_vector_type(4)));
typedef float f32x4 __attribute__((ext_vector_type(4)));
typedef float f32x2 __attribute__((ext_vector_type(2)));
typedef short s16x4 __attribute__((ext_vector_type(4)));
typedef __bf16 bf16x2 __attribute__((ext_vector_type(2)));
typedef __bf16 bf16x8 __attribute__((ext_vector_type(8)));
#define GAS __attribute__((address_space(1)))
#define LAS __attribute__((address_space(3)))
#define CAS __attribute__((address_space(4)))

__device__ __forceinline__ float u2f(unsigned x) { return __builtin_bit_cast(float, x); }
__device__ __forceinline__ unsigned f2u(float x) { return __builtin_bit_cast(unsigned, x); }
template <int DC> __device__ __forceinline__ float dpp(float x) {
  return __builtin_bit_cast(float, __builtin_amdgcn_mov_dpp(__builtin_bit_cast(int, x), DC, 0xf, 0xf, true));
}
__device__ __forceinline__ float red16_max(float x) {
  x = fmaxf(x, dpp<0x124>(x));
  x = fmaxf(x, dpp<0x128>(x));
  auto s = __builtin_amdgcn_permlane16_swap(f2u(x), f2u(x), false, false);
  x = fmaxf(u2f(s[0]), u2f(s[1]));
  auto t = __builtin_amdgcn_permlane32_swap(f2u(x), f2u(x), false, false);
  return fmaxf(u2f(t[0]), u2f(t[1]));
}
__device__ __forceinline__ float red16_sum(float x) {
  x += dpp<0x124>(x);
  x += dpp<0x128>(x);
  auto s = __builtin_amdgcn_permlane16_swap(f2u(x), f2u(x), false, false);
  x = u2f(s[0]) + u2f(s[1]);
  auto t = __builtin_amdgcn_permlane32_swap(f2u(x), f2u(x), false, false);
  return u2f(t[0]) + u2f(t[1]);
}
__device__ __forceinline__ unsigned pack_bf16(float lo, float hi) {
  bf16x2 r; r[0] = (__bf16)lo; r[1] = (__bf16)hi;
  return __builtin_bit_cast(unsigned, r);
}
__device__ __forceinline__ u32x4 make_srd(const void* p, unsigned bytes) {
  const unsigned long long a = (unsigned long long)p;
  return u32x4{(unsigned)a, (unsigned)(a >> 32) & 0xFFFFu, bytes, 0x00020000u};
}
__device__ __forceinline__ u32x4 tag4(f32x4 v, unsigned tag) {
  return u32x4{__builtin_amdgcn_perm(f2u(v[0]), tag, 0x07060500u), __builtin_amdgcn_perm(f2u(v[1]), tag, 0x07060501u),
               __builtin_amdgcn_perm(f2u(v[2]), tag, 0x07060502u), __builtin_amdgcn_perm(f2u(v[3]), tag, 0x07060503u)};
}
__device__ __forceinline__ unsigned untag4(u32x4 v) {
  return __builtin_amdgcn_perm(__builtin_amdgcn_perm(v[3], v[2], 0x0c0c0400u), __builtin_amdgcn_perm(v[1], v[0], 0x0c0c0400u), 0x05040100u);
}
#define SB() __builtin_amdgcn_sched_barrier(0)
#define STAMP(i) do {} while (0)
#define USTAMP(v, i) do {} while (0)
#define LDS_BUMP(off) asm volatile("s_mov_b64 exec, 1\n\tds_add_u32 %0, %1\n\ts_mov_b64 exec, -1" :: "v"((unsigned)(uintptr_t)lds + (off)), "v"(1) : "memory")
#define VMHIST(v) do {} while (0)
#define PACE_WAIT() do { \
    const unsigned vm0_ = __builtin_amdgcn_s_getreg(7 | (3 << 11));         \
    const int adj_ = vm0_ > VMTHR ? STRETCH : -min(debt, REPAY); \
    debt += adj_; tsched += pace + adj_; \
    int late_ = 0; \
    _Pragma("nounroll") for (int z_ = 0; z_ < 96; ++z_) { late_ = (int)(((unsigned)__builtin_amdgcn_s_memrealtime() << 4) - tsched); if (late_ >= 0) break; __builtin_amdgcn_s_sleep(1); } \
    VMHIST(vm0_); \
    late_ = max(late_ - XLATE, 0); debt += late_; tsched += late_; } while (0)
#define SPIN_FAST(cond) _Pragma("nounroll") for (int spin_ = 0; spin_ < SPIN_CAP && !(cond); ++spin_) __builtin_amdgcn_s_sleep(HSLEEP)
#define SPIN_UNTIL(cond) _Pragma("nounroll") for (int spin_ = 0; spin_ < SPIN_CAP && !(cond); ++spin_) __builtin_amdgcn_s_sleep(1)

constexpr int RROW = 1088;
constexpr int HALF = 4 * RROW;
constexpr int SCW = 64 * 256 + 1024;
constexpr int L_ARR = 0;
constexpr int L_CNT = 64;
constexpr int L_ML = L_CNT + 64;
constexpr int L_LIST = L_ML + B * 4 * NS * 8;
constexpr int L_SC = L_LIST + 68 * 4;
constexpr int L_RING = L_SC + NS * SCW;
constexpr int L_END = L_RING + NS * 4 * HALF;

__global__ void __launch_bounds__(NTHR, 2) attn_decode(const unsigned short* __restrict__ q, const void* __restrict__ kc, const void* __restrict__ vc,
                                                       const int* __restrict__ bt, const int* __restrict__ ctx, unsigned short* __restrict__ out,
                                                       unsigned char* __restrict__ ws, int nphys) {
  __shared__ __attribute__((aligned(16))) unsigned char lds_[L_END];
  LAS unsigned char* lds = (LAS unsigned char*)lds_;
  const int tid = threadIdx.x, lane = tid & 63, w = __builtin_amdgcn_readfirstlane(tid >> 6);
  const int g = lane >> 4, c = lane & 15, lq = c & 3;
  const int hg = (blockIdx.x >> 3) & 7, s = ((blockIdx.x & 7) << 2) | (blockIdx.x >> 6);
  const int plast = ((s & 3) << 3) | (((s >> 2) - hg) & 7);
  volatile LAS unsigned* arr = (volatile LAS unsigned*)(lds + L_ARR);
  unsigned cpf;
  asm volatile("global_load_dword %0, %1, %2" : "=v"(cpf) : "v"(min(lane * 128, CODEPF - 4)), "s"(__builtin_amdgcn_s_getpc() & ~255ull) : "memory");
  STAMP(0);
  const unsigned tstart = (unsigned)__builtin_amdgcn_s_memrealtime() << 4;
  asm volatile("" :: "s"(tstart), "s"(q), "s"(kc), "s"(vc), "s"(bt), "s"(ctx), "s"(out), "s"(ws), "s"(nphys));
  const unsigned empty = (unsigned)nphys << 2;

  if (__builtin_expect(w < NS, 1)) {
    i32x4 cl0 = *(const CAS i32x4*)ctx;
    int e00 = ((const CAS int*)bt)[s], e01 = ((const CAS int*)bt)[s + NSPLIT], e02 = ((const CAS int*)bt)[s + 2 * NSPLIT];
    u32x4 qf[4];
    int ev[B];
    asm volatile("global_load_dwordx4 %0, %8, %10\n\tglobal_load_dwordx4 %1, %8, %10 offset:64\n\t"
                 "global_load_dwordx4 %2, %8, %10 offset:128\n\tglobal_load_dwordx4 %3, %8, %10 offset:192\n\t"
                 "global_load_dword %4, %9, %11 offset:-4096\n\tglobal_load_dword %5, %9, %11 offset:-2048\n\t"
                 "global_load_dword %6, %9, %11\n\tglobal_load_dword %7, %9, %11 offset:2048"
                 : "=&v"(qf[0]), "=&v"(qf[1]), "=&v"(qf[2]), "=&v"(qf[3]), "=&v"(ev[0]), "=&v"(ev[1]), "=&v"(ev[2]), "=&v"(ev[3])
                 : "v"(((lq * H + hg * 4 + (c >> 2)) * D + 8 * g) * 2), "v"((2 * NB + s + NSPLIT * c) * 4), "s"(q), "s"(bt) : "memory");
    const u32x4 rk = make_srd(kc, (unsigned)nphys * BLKB), rv = make_srd(vc, (unsigned)nphys * BLKB);
    int voff[4];
#pragma unroll
    for (int i = 0; i < 4; ++i) voff[i] = i * (TOKB - RROW) + hg * 1024 + lane * 16;
    const unsigned ring = (unsigned)(uintptr_t)lds + L_RING + w * 4 * HALF;
    asm volatile("" : "+v"(voff[0]), "+v"(voff[1]), "+v"(voff[2]), "+v"(voff[3]) : "s"(rk), "s"(ring));
    asm volatile("" : "+s"(e00), "+s"(e01), "+s"(e02), "+s"(cl0));
#define DMA4(rs, so, dst) asm volatile("s_mov_b32 m0, %0\n\ts_nop 3\n\t" \
      "buffer_load_dwordx4 %1, %5, %6 offen nt lds\n\t" \
      "buffer_load_dwordx4 %2, %5, %6 offen offset:1088 nt lds\n\t" \
      "buffer_load_dwordx4 %3, %5, %6 offen offset:2176 nt lds\n\t" \
      "buffer_load_dwordx4 %4, %5, %6 offen offset:3264 nt lds" \
      :: "s"(dst), "v"(voff[0]), "v"(voff[1]), "v"(voff[2]), "v"(voff[3]), "s"(rs), "s"(so) : "memory")
    DMA4(rk, ((min((unsigned)e00, (unsigned)nphys) << 2) | (unsigned)w) << 15, ring);
    DMA4(rk, ((min((unsigned)e01, (unsigned)nphys) << 2) | (unsigned)w) << 15, ring + HALF);
    DMA4(rk, ((min((unsigned)e02, (unsigned)nphys) << 2) | (unsigned)w) << 15, ring + 2 * HALF);
#define QWAIT "12"
#define WPRE "8"
#define WPOST "12"
    STAMP(1);
    const int stag = STAG ? (((hg * 4 + (s & 3)) * 13 & 31) * PACE) / 32 - PACE / 2 : 0;
    unsigned tsched = tstart + PACE0 + (LEAD - 1) * PACE + stag, pace = PACE;
    int debt = 0;
    __builtin_amdgcn_s_barrier();
    int cl[B];
#pragma unroll
    for (int b = 0; b < B; ++b) cl[b] = (cl0[b] <= 0 || cl0[b] > NB * BS) ? NB * BS : cl0[b];
    asm volatile("s_waitcnt vmcnt(" QWAIT ")" : "+v"(cpf), "+v"(qf[0]), "+v"(qf[1]), "+v"(qf[2]), "+v"(qf[3]), "+v"(ev[0]), "+v"(ev[1]), "+v"(ev[2]), "+v"(ev[3]) :: "memory");
    unsigned descv;
    {
      const int j = s + NSPLIT * c;
      unsigned mk = 1u;
#pragma unroll
      for (int b = 1; b < B; ++b) mk |= (j * BS < cl[b] && ev[b] == ev[0]) ? (1u << b) : 0u;
      descv = (unsigned)w | (min((unsigned)ev[0], (unsigned)nphys) << 2) | ((unsigned)j << 17) | (mk << 28);
      descv = j * BS < cl[0] ? descv : empty;
    }
    const int cq0 = (lq & 2) ? ((lq & 1) ? cl0[3] : cl0[2]) : ((lq & 1) ? cl0[1] : cl0[0]);
#pragma unroll
    for (int x = 0; x < 4; ++x) if (cq0 <= 0) qf[x] = u32x4{0u, 0u, 0u, 0u};
    const bool diag = (c >> 2) == g;
    const int ctxq = !diag ? 0 : (cq0 <= 0 || cq0 > NB * BS) ? NB * BS : cq0;
    const LAS unsigned char* kp = lds + L_RING + w * 4 * HALF + (c & 3) * RROW + (c >> 2) * 256 + g * 16;
    const LAS unsigned char* vp = lds + L_RING + w * 4 * HALF + (c >> 2) * RROW + g * 256 + (c & 3) * 8;
    LAS unsigned char* scp = lds + L_SC + w * SCW + (diag ? c * 16 : 64 * 256 + lane * 16);
    const int scs = diag ? 256 : 0;

    f32x4 acc[8];
#pragma unroll
    for (int j = 0; j < 8; ++j) acc[j] = f32x4{0.f, 0.f, 0.f, 0.f};
    float m = MINIT, l = 0.f;
    int n = 0x10000;

    auto scores = [&](int slot, unsigned d) -> f32x4 {
      u32x4 kf[4];
#pragma unroll
      for (int x = 0; x < 4; ++x) kf[x] = *(const LAS u32x4*)(kp + slot * HALF + 64 * x);
      f32x4 sc = {0.f, 0.f, 0.f, 0.f};
#pragma unroll
      for (int x = 0; x < 4; ++x)
        sc = __builtin_amdgcn_mfma_f32_16x16x32_bf16(__builtin_bit_cast(bf16x8, kf[x]), __builtin_bit_cast(bf16x8, qf[x]), sc, 0, 0, 0);
      const int tok0 = (int)((d >> 17) & 511u) * BS + (int)(d & 3u) * 4;
      int lim = ((d >> (28 + lq)) & 1u) ? ctxq - tok0 : 0;
      asm volatile("" : "+v"(lim));
#pragma unroll
      for (int i = 0; i < 4; ++i) sc[i] = (i < lim) ? sc[i] : -INFINITY;
      return sc;
    };
    auto need_list = [&](int pos) {
      if (n == 0x10000 && (pos >= EARLY || (__builtin_amdgcn_readlane(descv, pos) >> 28) == 0u)) {
        SPIN_UNTIL(__builtin_amdgcn_readfirstlane(arr[6]) == 4u);
        const int total = __builtin_amdgcn_readfirstlane(arr[7]);
        int skip = 0;
#pragma unroll
        for (int k = 0; k < LEAD; ++k) skip += (__builtin_amdgcn_readlane(descv, k) >> 28) == 0u;
        const int mine = total / NSPLIT + ((total >= EARLY * NSPLIT ? plast : s) < total % NSPLIT ? 1 : 0);
        const unsigned d = *(const LAS unsigned*)(lds + L_LIST + max(lane - skip, 0) * 4) | (unsigned)w;
        descv = lane < LEAD ? descv : (lane - skip < mine) ? d : empty;
        n = max(pos, mine + skip);
        const unsigned tlast = (COMMON ? __builtin_amdgcn_readfirstlane(arr[5]) : tstart) + PACE0 + stag + (unsigned)(2 * PACE * total) / NSPLIT - PACE;
        pace = (unsigned)max((int)(tlast - tsched), 0) / (unsigned)(2 * n - pos);
        debt = 0;
        STAMP(6);
      }
    };

    need_list(LEAD);
    for (int i = 0; i < n; ++i) {
      const bool isv = i + LEAD >= n;
      const unsigned so = (unsigned)__builtin_amdgcn_readlane(descv, isv ? i + LEAD - n : i + LEAD) << 15;
      const u32x4 rs = {isv ? rv[0] : rk[0], isv ? rv[1] : rk[1], rk[2], rk[3]};
      PACE_WAIT();
      asm volatile("s_waitcnt vmcnt(" WPRE ")" ::: "memory");
      DMA4(rs, so, ring + ((i + LEAD) & 3) * HALF);
      const f32x4 sc = scores(i & 3, __builtin_amdgcn_readlane(descv, i));
      m = fmaxf(m, fmaxf(fmaxf(sc[0], sc[1]), fmaxf(sc[2], sc[3])) * C2);
      *(LAS f32x4*)(scp + i * scs) = sc;
      need_list(i + LEAD + 1);
    }
    STAMP(10);
    int qdone = 0;
    for (int i = 0; i < n; ++i) {
      const unsigned so = (unsigned)__builtin_amdgcn_readlane(descv, (i + LEAD) & 63) << 15;
      const f32x4 sc = *(const LAS f32x4*)(scp + i * scs);
      float p[4];
#pragma unroll
      for (int k = 0; k < 4; ++k) p[k] = __builtin_amdgcn_exp2f(fmaf(sc[k], C2, -m));
      l += (p[0] + p[1]) + (p[2] + p[3]);
      u32x2 a = {pack_bf16(p[0], p[1]), pack_bf16(p[2], p[3])};
      asm volatile("" : "+v"(a));
      if (i + LEAD < n) {
        if (i + LEAD + SPRINT < n) PACE_WAIT();
        asm volatile("s_waitcnt vmcnt(" WPRE ")" ::: "memory");
        DMA4(rv, so, ring + ((n + i + LEAD) & 3) * HALF);
      } else if (i + 1 == n) asm volatile("s_waitcnt vmcnt(0)" ::: "memory");
      else if (i + 2 == n) asm volatile("s_waitcnt vmcnt(4)" ::: "memory");
      else asm volatile("s_waitcnt vmcnt(8)" ::: "memory");
      const LAS unsigned char* sp = vp + ((n + i) & 3) * HALF;
#pragma unroll
      for (int j = 0; j < 8; ++j)
        acc[j] = __builtin_amdgcn_mfma_f32_16x16x16bf16_1k(__builtin_bit_cast(s16x4, a), __builtin_amdgcn_ds_read_tr16_b64_v4i16((LAS s16x4*)(sp + 32 * j)), acc[j], 0, 0, 0);
      if (i == max(n - PUBLEAD, 0)) LDS_BUMP(L_ARR + 36);
      const unsigned mnext = (unsigned)__builtin_amdgcn_readlane(descv, (i + 1) & 63) >> 28;
      const int qlim = i + 1 == n ? B : min(mnext ? __builtin_ctz(mnext) : 0, (i + 1) >> 3);
      while (qdone < qlim) {
        const int qq = qdone++;
        int qsel = qq; asm volatile("" : "+v"(qsel));
        const bool b0 = qsel & 1, b1 = qsel & 2;
        LAS float* dst = (LAS float*)(lds + L_SC + w * SCW) + (qq * 4 + g) * D + c;
#pragma unroll
        for (int j = 0; j < 8; ++j) { const float lo = b0 ? acc[j][1] : acc[j][0], hi = b0 ? acc[j][3] : acc[j][2]; dst[16 * j] = b1 ? hi : lo; }
        if (c == 4 * g + qq) *(LAS f32x2*)(lds + L_ML + ((qq * 4 + g) * NS + w) * 8) = f32x2{m, l};
        LDS_BUMP(L_ARR + qq * 4);
      }
    }
    STAMP(11);

    STAMP(3);
    const auto rwb = __builtin_amdgcn_make_buffer_rsrc((void*)ws, 0, WS_END, 0x00020000);
    const int gg = s >> 3, slice = s & 7, pp = lane >> 2;
    const int row0 = ((hg * 4 + w) * 4 + gg) * NSPLIT;
    const int ao = WS_PO + (row0 + pp) * 512 + slice * 64 + lq * 16, am = WS_ML + (row0 + pp) * 16;
    const unsigned tag = __builtin_amdgcn_readfirstlane(arr[4]);
    SPIN_UNTIL(__builtin_amdgcn_readfirstlane(arr[B - 1]) == (unsigned)NS);
    __builtin_amdgcn_s_sleep(POLLDELAY);
    u32x4 gv[2], gm[2];
    bool ok = false;
    _Pragma("nounroll") for (int spin = 0; spin < SPIN_CAP; ++spin) {
      if (!ok) {
#pragma unroll
        for (int i = 0; i < 2; ++i) {
          gv[i] = __builtin_amdgcn_raw_buffer_load_b128(rwb, ao + i * 16 * 512, 0, 16);
          gm[i] = __builtin_amdgcn_raw_buffer_load_b128(rwb, am + i * 16 * 16, 0, 16);
        }
        unsigned bad = 0;
#pragma unroll
        for (int i = 0; i < 2; ++i) bad |= (untag4(gv[i]) ^ tag) | (gm[i][1] ^ tag) | (gm[i][3] ^ tag);
        ok = bad == 0;
      }
      if (__builtin_amdgcn_ballot_w64(!ok) == 0) break;
    }
    STAMP(4);
    const float M = red16_max(fmaxf(u2f(gm[0][0]), u2f(gm[1][0])));
    const float w0 = __builtin_amdgcn_exp2f(u2f(gm[0][0]) - M), w1 = __builtin_amdgcn_exp2f(u2f(gm[1][0]) - M);
    const float L = red16_sum(w0 * u2f(gm[0][2]) + w1 * u2f(gm[1][2]));
    float o[4];
#pragma unroll
    for (int k = 0; k < 4; ++k) o[k] = red16_sum(w0 * u2f(gv[0][k] & 0xFFFFFF00u) + w1 * u2f(gv[1][k] & 0xFFFFFF00u));
    const float inv = 1.f / L;
    if (lane < 4)
      *(u32x2*)(out + (w * H + hg * 4 + gg) * D + slice * 16 + lane * 4) = u32x2{pack_bf16(o[0] * inv, o[1] * inv), pack_bf16(o[2] * inv, o[3] * inv)};
    if (s == 0 && w == 0 && lane == 0)
      __hip_atomic_store((GAS unsigned*)(ws + WS_EPOCH + hg * 256), tag, __ATOMIC_RELAXED, __HIP_MEMORY_SCOPE_AGENT);
  } else {
    const int h = w - NS, tcol = (h * 64 + lane) * 2;
    const i32x4 cl0 = *(const CAS i32x4*)ctx;
    i32x2 tb[B];
#pragma unroll
    for (int b = 0; b < B; ++b) tb[b] = *(const i32x2*)(bt + b * NB + tcol);
    GAS unsigned* pe = (GAS unsigned*)(ws + WS_EPOCH + hg * 256);
    const unsigned ev = __hip_atomic_load(pe, __ATOMIC_RELAXED, __HIP_MEMORY_SCOPE_AGENT);
    GAS unsigned* pst = (GAS unsigned*)(ws + WS_START);
    if (h == 1 && lane == 0 && blockIdx.x < 64) __hip_atomic_store(pst + blockIdx.x, tstart, __ATOMIC_RELAXED, __HIP_MEMORY_SCOPE_AGENT);
    if (h == 0 && lane < 16) arr[lane] = 0u;
    asm volatile("s_waitcnt lgkmcnt(0)" ::: "memory");
    SB();
    __builtin_amdgcn_s_barrier();
    SB();
    const unsigned e32 = __builtin_amdgcn_readfirstlane(ev);
    const unsigned tag = ((e32 & 0x7F7F7F7Fu) + 0x01010101u) ^ (e32 & 0x80808080u);
    int cl[B];
#pragma unroll
    for (int b = 0; b < B; ++b) cl[b] = (cl0[b] <= 0 || cl0[b] > NB * BS) ? NB * BS : cl0[b];
    unsigned msk[B][2]; int pre[B];
    unsigned cntv = 0;
#pragma unroll
    for (int b = 0; b < B; ++b) {
      int below = 0, cnt = 0;
#pragma unroll
      for (int e = 0; e < 2; ++e) {
        const int j16 = (tcol + e) * BS;
        bool own = j16 < cl[b];
        unsigned mk = 1u << b;
#pragma unroll
        for (int o = 0; o < B; ++o) {
          if (o == b) continue;
          const bool same = (j16 < cl[o]) && (tb[o][e] == tb[b][e]);
          if (o < b) own = own && !same; else mk |= same ? (1u << o) : 0u;
        }
        msk[b][e] = own ? mk : 0u;
        const unsigned long long bal = __builtin_amdgcn_ballot_w64(own);
        below = __builtin_amdgcn_mbcnt_hi((unsigned)(bal >> 32), __builtin_amdgcn_mbcnt_lo((unsigned)bal, below));
        cnt += __builtin_popcountll(bal);
      }
      pre[b] = below;
      cntv = (lane == b) ? (unsigned)cnt : cntv;
    }
    if (lane < B) *(LAS unsigned*)(lds + L_CNT + (h * B + lane) * 4) = cntv;
    LDS_BUMP(L_ARR + 32);
    SPIN_UNTIL(__builtin_amdgcn_readfirstlane(arr[8]) == 4u);
    asm volatile("" ::: "memory");
    {
      int tot = 0, all = 0;
      u32x4 cw[4];
#pragma unroll
      for (int x = 0; x < 4; ++x) { cw[x] = *(const LAS u32x4*)(lds + L_CNT + x * 16); all += (int)(cw[x][0] + cw[x][1] + cw[x][2] + cw[x][3]); }
#pragma unroll
      for (int b = 0; b < B; ++b) {
        int base = tot + pre[b];
#pragma unroll
        for (int x = 0; x < 4; ++x) { base += (x < h) ? (int)cw[x][b] : 0; tot += (int)cw[x][b]; }
#pragma unroll
        for (int e = 0; e < 2; ++e) {
          const unsigned ph = min((unsigned)tb[b][e], (unsigned)nphys);
          const int want = (base / NSPLIT == all / NSPLIT && all >= EARLY * NSPLIT) ? plast : s;
          const int slot = (msk[b][e] && (base & (NSPLIT - 1)) == want) ? base / NSPLIT : 64;
          *(LAS unsigned*)(lds + L_LIST + slot * 4) = (ph << 2) | ((unsigned)(tcol + e) << 17) | (msk[b][e] << 28);
          base += msk[b][e] ? 1 : 0;
        }
      }
      if (h == 0 && lane == 0) arr[7] = (unsigned)tot;
    }
    if (h == 0) {
      int dmean = 0;
      _Pragma("nounroll") for (int z = 0; z < 64 && (int)(((unsigned)__builtin_amdgcn_s_memrealtime() << 4) - tstart) < MEANAT; ++z) __builtin_amdgcn_s_sleep(4);
      const int dt = (int)(__hip_atomic_load(pst + lane, __ATOMIC_RELAXED, __HIP_MEMORY_SCOPE_AGENT) - tstart);
      const bool valid = dt > -4000 && dt < 4000;
      float sum = valid ? (float)dt : 0.f;
      sum += dpp<0xB1>(sum); sum += dpp<0x4E>(sum); sum = red16_sum(sum);
      dmean = (int)(sum / (float)max(__builtin_popcountll(__builtin_amdgcn_ballot_w64(valid)), 1));
      if (lane == 0) { arr[4] = tag; arr[5] = tstart + dmean; }
    }
    asm volatile("s_waitcnt vmcnt(0)" : "+v"(cpf) : "s"(tag) : "memory");
    LDS_BUMP(L_ARR + 24);
    STAMP(1);

    const auto rw = __builtin_amdgcn_make_buffer_rsrc((void*)ws, 0, WS_END, 0x00020000);
    const int rec = ((hg * 4 + h) * 4 + g) * NSPLIT + s;
    const int po = WS_PO + rec * 512 + c * 16;
    SPIN_FAST(__builtin_amdgcn_readfirstlane(arr[h]) == (unsigned)NS);
    asm volatile("" ::: "memory");
    STAMP(2);
    f32x2 ml[NS]; f32x4 d0[NS], d1[NS];
#pragma unroll
    for (int x = 0; x < NS; ++x) {
      ml[x] = *(const LAS f32x2*)(lds + L_ML + ((h * 4 + g) * NS + x) * 8);
      const LAS float* src = (const LAS float*)(lds + L_SC + x * SCW) + (h * 4 + g) * D + 4 * c;
      d0[x] = *(const LAS f32x4*)src; d1[x] = *(const LAS f32x4*)(src + 64);
    }
    float M = ml[0][0];
#pragma unroll
    for (int x = 1; x < NS; ++x) M = fmaxf(M, ml[x][0]);
    float L = 0.f;
    f32x4 o0 = {0.f, 0.f, 0.f, 0.f}, o1 = {0.f, 0.f, 0.f, 0.f};
#pragma unroll
    for (int x = 0; x < NS; ++x) {
      const float wt = __builtin_amdgcn_exp2f(ml[x][0] - M);
      L = fmaf(wt, ml[x][1], L);
      o0 += wt * d0[x]; o1 += wt * d1[x];
    }
    u32x4 t0 = tag4(o0, tag), t1 = tag4(o1, tag);
    asm volatile("" : "+v"(t0), "+v"(t1));
    if (h < B - 1) SPIN_UNTIL(__builtin_amdgcn_readfirstlane(arr[9]) == (unsigned)NS);
    __builtin_amdgcn_raw_buffer_store_b128(t0, rw, po, 0, 16);
    __builtin_amdgcn_raw_buffer_store_b128(t1, rw, po + 256, 0, 16);
    __builtin_amdgcn_raw_buffer_store_b128(u32x4{f2u(M), tag, f2u(L), tag}, rw, c == 0 ? WS_ML + rec * 16 : OOB, 0, 16);
    STAMP(3);
  }
}
}

extern "C" void kernel_launch(void* const* d_in, const int* in_sizes, int n_in, void* d_out, int out_size, void* d_ws, size_t ws_size, hipStream_t stream) {
  const int nphys = in_sizes[1] / (BS * H * D);
  hipLaunchKernelGGL(attn_decode, dim3(NWG), dim3(NTHR), 0, stream, (const unsigned short*)d_in[0], d_in[1], d_in[2], (const int*)d_in[3], (const int*)d_in[4],
                     (unsigned short*)d_out, (unsigned char*)d_ws, nphys);
}
```
